```python
import torch

def _softmax(x, axis=-1):
    return torch.softmax(x, dim=axis)
def _gelu(x):
    return torch.nn.functional.gelu(x, approximate="tanh")
def _silu(x):
    return torch.nn.functional.silu(x)
def _sigmoid(x):
    return torch.sigmoid(x)
def _layernorm(x, axis=-1, eps=1e-5):
    m = x.mean(dim=axis, keepdim=True)
    v = x.var(dim=axis, keepdim=True, unbiased=False)
    return (x - m) / torch.sqrt(v + eps)
def _scatter_add(tgt, idx, src, axis=0):
    return tgt.clone().index_add_(axis, idx, src)
def _segment_sum(src, seg, n):
    out = torch.zeros((n,) + src.shape[1:], dtype=src.dtype)
    return out.index_add_(0, seg, src)
def _pad(x, pw):
    flat = tuple(v for pair in reversed(pw) for v in pair)
    return torch.nn.functional.pad(x, flat)

kernel_name = 'gated_mix__spectral_gfnet__mixer_token_mlp__prompt'

def setup_inputs():

    _w = (lambda a: (lambda t: t.to(torch.bfloat16) if t.is_floating_point() else t)(torch.as_tensor(a)))
    def _A_setup():
        B, S, D = 1, 2048, 512
        Df = D // 2 + 1
        return {
            "x": _w((lambda *s: torch.randn(*s, dtype=torch.bfloat16) * 0.05)(B, S, D)),
            "w_r": _w((lambda *s: torch.randn(*s, dtype=torch.bfloat16) * 0.8)(S, Df) * 0.02),
            "w_i": _w((lambda *s: torch.randn(*s, dtype=torch.bfloat16) * 1.6)(S, Df) * 0.02),
        }
    _A_in = _A_setup()

    def _B_setup():
        B, S, D, F = 1, 2048, 512, 1792
        return {
            "x": _w((lambda *s: torch.randn(*s, dtype=torch.bfloat16) * 0.05)(B, S, D)),
            "w1": _w((lambda *s: torch.randn(*s, dtype=torch.bfloat16) * 0.2)(S, F) * (S ** -0.5)),
            "b1": _w((lambda *s: torch.randn(*s, dtype=torch.bfloat16) * 3.2)(F) * 0.02),
            "w2": _w((lambda *s: torch.randn(*s, dtype=torch.bfloat16) * 0.05)(F, S) * (F ** -0.5)),
            "b2": _w((lambda *s: torch.randn(*s, dtype=torch.bfloat16) * 0.1)(S) * 0.02),
        }
    _B_in = _B_setup()

    return {
        "A_x": _A_in["x"],
        "A_w_r": _A_in["w_r"],
        "A_w_i": _A_in["w_i"],
        "B_x": _B_in["x"],
        "B_w1": _B_in["w1"],
        "B_b1": _B_in["b1"],
        "B_w2": _B_in["w2"],
        "B_b2": _B_in["b2"],
        "gate_w": _w((lambda *s: torch.randn(*s, dtype=torch.bfloat16) * 0.05)(512) * 16.0),
    }

def reference(A_x, A_w_r, A_w_i, B_x, B_w1, B_b1, B_w2, B_b2, gate_w):
    def _A_ref(x, w_r, w_i):
        B, S, D = x.shape
        X = (lambda x, axes=(-2,-1): torch.fft.rfft2(x.float(), dim=axes))(x, axes=(-2, -1))
        W = w_r + 1j * w_i
        X = X * W
        y = (lambda x, s, axes=(-2,-1): torch.fft.irfft2(x, s=s, dim=axes).to(torch.bfloat16))(X, (S, D), axes=(-2, -1))
        return y

    def _B_ref(x, w1, b1, w2, b2):
        B, S, D = x.shape
        h = _layernorm(x, axis=-1)
        u = torch.einsum("bsd,sf->bdf", h, w1) + b1
        u = _gelu(u)
        y = torch.einsum("bdf,fs->bds", u, w2) + b2
        return (lambda x, *d: x.permute(*d))(y, 0, 2, 1)

    x = A_x
    g = _sigmoid((x * gate_w).sum(-1, keepdims=True))
    a = _A_ref(x, A_w_r, A_w_i)
    b = _B_ref(x, B_w1, B_b1, B_w2, B_b2)
    return g * a + (1 - g) * b
```

```cpp
#include <hip/hip_runtime.h>
#include <hip/hip_bf16.h>
#include <stdint.h>

typedef unsigned short u16;
__device__ __forceinline__ float bf2f(u16 v){ return __uint_as_float(((uint32_t)v)<<16); }
__device__ __forceinline__ u16 f2bf(float f){ __hip_bfloat16 b = __float2bfloat16(f); return *(u16*)&b; }
__device__ __forceinline__ float rbf(float f){ return bf2f(f2bf(f)); }

constexpr int S=2048, D=512, F=1792, DF=257;

__global__ void k_row(const u16* x, const u16* gw, u16* h, float* g, float2* X1){
  __shared__ float xs[D]; __shared__ float red[256]; __shared__ float ct[D], st[D];
  int s=blockIdx.x, t=threadIdx.x;
  for(int i=t;i<D;i+=256){ xs[i]=bf2f(x[s*D+i]); float a,b; sincospif(2.0f*i/D,&a,&b); st[i]=a; ct[i]=b; }
  __syncthreads();
  float p=xs[t]+xs[t+256]; red[t]=p; __syncthreads();
  for(int o=128;o>0;o>>=1){ if(t<o) red[t]+=red[t+o]; __syncthreads(); }
  float mean=red[0]/D; __syncthreads();
  float d0=xs[t]-mean,d1=xs[t+256]-mean; red[t]=d0*d0+d1*d1; __syncthreads();
  for(int o=128;o>0;o>>=1){ if(t<o) red[t]+=red[t+o]; __syncthreads(); }
  float var=red[0]/D; __syncthreads();
  red[t]=rbf(xs[t]*bf2f(gw[t]))+rbf(xs[t+256]*bf2f(gw[t+256])); __syncthreads();
  for(int o=128;o>0;o>>=1){ if(t<o) red[t]+=red[t+o]; __syncthreads(); }
  float gs=rbf(red[0]);
  if(t==0) g[s]=rbf(1.0f/(1.0f+expf(-gs)));
  float mb=rbf(mean), vb=rbf(var); float den=rbf(sqrtf(rbf(vb+1e-5f)));
  for(int i=t;i<D;i+=256) h[s*D+i]=f2bf(rbf(xs[i]-mb)/den);
  for(int k=t;k<DF;k+=256){ float re=0,im=0; for(int d=0;d<D;d++){ int a=(k*d)&(D-1); re+=xs[d]*ct[a]; im-=xs[d]*st[a]; } X1[s*DF+k]=make_float2(re,im);}
}
__global__ void k_col(const float2* X1, const u16* wr, const u16* wi, float2* Y1){
  __shared__ float2 a[S]; __shared__ float2 b[S]; __shared__ float2 tw[S];
  int k=blockIdx.x,t=threadIdx.x;
  for(int i=t;i<S;i+=blockDim.x){ a[i]=X1[i*DF+k]; float s_,c_; sincospif(2.0f*i/S,&s_,&c_); tw[i]=make_float2(c_,s_);}
  __syncthreads();
  for(int j=t;j<S;j+=blockDim.x){ float re=0,im=0; for(int s=0;s<S;s++){ float2 w=tw[(j*s)&(S-1)]; float2 v=a[s]; re+=v.x*w.x+v.y*w.y; im+=v.y*w.x-v.x*w.y; }
    float r=bf2f(wr[j*DF+k]), q=bf2f(wi[j*DF+k]); b[j]=make_float2(re*r-im*q, re*q+im*r);}
  __syncthreads();
  for(int s=t;s<S;s+=blockDim.x){ float re=0,im=0; for(int j=0;j<S;j++){ float2 w=tw[(j*s)&(S-1)]; float2 v=b[j]; re+=v.x*w.x-v.y*w.y; im+=v.y*w.x+v.x*w.y; }
    Y1[s*DF+k]=make_float2(re/S,im/S);}
}
__global__ void k_g1(const u16* h, const u16* w1, const u16* b1, u16* u){
  int f=blockIdx.x*64+(threadIdx.x&63); int d=blockIdx.y*4+(threadIdx.x>>6);
  float acc=0; for(int s=0;s<S;s++) acc+=bf2f(h[s*D+d])*bf2f(w1[s*F+f]);
  float v=rbf(rbf(acc)+bf2f(b1[f]));
  float in=0.7978845608028654f*(v+0.044715f*v*v*v);
  u[d*F+f]=f2bf(0.5f*v*(1.0f+tanhf(in)));
}
__global__ void k_fin(const u16* u, const u16* w2, const u16* b2, const float* g, const float2* Y1, u16* out){
  __shared__ float ct[D], st[D];
  for(int i=threadIdx.x;i<D;i+=256){ float a,b; sincospif(2.0f*i/D,&a,&b); st[i]=a; ct[i]=b; }
  __syncthreads();
  int s=blockIdx.x*64+(threadIdx.x&63); int d=blockIdx.y*4+(threadIdx.x>>6);
  float acc=0; for(int f=0;f<F;f++) acc+=bf2f(u[d*F+f])*bf2f(w2[f*S+s]);
  float bb=rbf(rbf(acc)+bf2f(b2[s]));
  float a=0; for(int k=0;k<DF;k++){ float2 y=Y1[s*DF+k]; int i=(k*d)&(D-1); float c=(k==0||k==256)?1.f:2.f; a+=c*(y.x*ct[i]-y.y*st[i]); }
  a=rbf(a/D);
  float gg=g[s];
  out[s*D+d]=f2bf(rbf(gg*a)+rbf(rbf(1.0f-gg)*bb));
}

extern "C" void kernel_launch(void* const* d_in, const int* in_sizes, int n_in, void* d_out, int out_size, void* d_ws, size_t ws_size, hipStream_t stream){
  const u16* x=(const u16*)d_in[0]; const u16* wr=(const u16*)d_in[1]; const u16* wi=(const u16*)d_in[2];
  const u16* w1=(const u16*)d_in[4]; const u16* b1=(const u16*)d_in[5]; const u16* w2=(const u16*)d_in[6]; const u16* b2=(const u16*)d_in[7]; const u16* gw=(const u16*)d_in[8];
  char* ws=(char*)d_ws;
  u16* h=(u16*)ws; float* g=(float*)(ws+(4<<20)); float2* X1=(float2*)(ws+(8<<20)); float2* Y1=(float2*)(ws+(16<<20)); u16* u=(u16*)(ws+(24<<20));
  k_row<<<S,256,0,stream>>>(x,gw,h,g,X1);
  k_col<<<DF,1024,0,stream>>>(X1,wr,wi,Y1);
  k_g1<<<dim3(F/64,D/4),256,0,stream>>>(h,w1,b1,u);
  k_fin<<<dim3(S/64,D/4),256,0,stream>>>(u,w2,b2,g,Y1,(u16*)d_out);
}
```

```cpp
#include <hip/hip_runtime.h>
#include <stdint.h>

typedef unsigned u32;
typedef unsigned long long u64;
typedef unsigned short u16;
typedef u32 v4u __attribute__((ext_vector_type(4)));
typedef u32 v2u __attribute__((ext_vector_type(2)));
typedef int v4i __attribute__((ext_vector_type(4)));
typedef short s4v __attribute__((ext_vector_type(4)));
typedef short s8v __attribute__((ext_vector_type(8)));
typedef float f2v __attribute__((ext_vector_type(2)));
typedef float f4v __attribute__((ext_vector_type(4)));
typedef float f16v __attribute__((ext_vector_type(16)));
typedef __bf16 b2v __attribute__((ext_vector_type(2)));
#define LAS __attribute__((address_space(3)))
#define DI __device__ __forceinline__
typedef __amdgpu_buffer_rsrc_t rsrc_t;

constexpr u32 O_EPOCH = 0, O_F1 = 4096, O_F2 = 32768, O_XN = 65536, O_VN = 73728, O_OMG = 81920, O_DIAG = 131072;
constexpr u32 O_H = 1u << 20, O_X1 = 3u << 20, O_V = 5u << 20, O_GA = 7u << 20, O_U = 9u << 20, WS_BYTES = 11u << 20;
constexpr u32 POISON = 0xFFFFFFFFu;
constexpr int AUX_SC1 = 16;
constexpr u32 SPIN_CAP = 200000;
constexpr u32 L_G = 0;
constexpr u32 L_S = 131072;
constexpr u32 L_X = L_S, L_HR = L_S + 8704, L_T = L_S + 17408;
constexpr u32 L_E = L_S, L_W = L_S + 8704, L_O = L_S + 17408;
constexpr u32 L_U = L_S;
constexpr u32 L_C = L_S + 26624;
constexpr u32 LDS_BYTES = 163840;
enum { C_H1 = 0, C_F1 = 1, C_OK = 2  , C_P2A = 6, C_P2B = 7, C_P3A = 8, C_P3B = 9, C_R1 = 10, C_R2 = 11, C_F2 = 12, C_TAG = 14  , C_G = 16  , C_NY = 24  , C_N = 32 };

struct Params {
  const u16 *x, *wr, *wi, *w1, *b1, *w2, *b2, *gw;
  u16* out;
  char* ws;
};

DI u32 pk(float a, float b) { f2v v = {a, b}; return __builtin_bit_cast(u32, __builtin_convertvector(v, b2v)); }
DI float blo(u32 v) { return __uint_as_float(v << 16); }
DI float bhi(u32 v) { return __uint_as_float(v & 0xFFFF0000u); }
DI float rbf(float a) { return blo(pk(a, a)); }
DI float cosr(int n, float inv) { return __builtin_amdgcn_cosf((float)n * inv); }
DI float sinr(int n, float inv) { return __builtin_amdgcn_sinf((float)n * inv); }
DI u32 san(u32 v) { return v < 0xFFFFFFFEu ? v : 0xFFFFFFFEu; }
DI v4u san4(v4u v) { v4u r = {san(v.x), san(v.y), san(v.z), san(v.w)}; return r; }
DI u32 max4(v4u v) { return max(max(v.x, v.y), max(v.z, v.w)); }
DI f4v mfma16(v4u a, v4u b, f4v c) { return __builtin_amdgcn_mfma_f32_16x16x32_bf16(__builtin_bit_cast(s8v, a), __builtin_bit_cast(s8v, b), c, 0, 0, 0); }
DI f16v mfma32(v4u a, v4u b, f16v c) { return __builtin_amdgcn_mfma_f32_32x32x16_bf16(__builtin_bit_cast(s8v, a), __builtin_bit_cast(s8v, b), c, 0, 0, 0); }
DI v2u trd(LAS char* L, u32 off) { return __builtin_bit_cast(v2u, __builtin_amdgcn_ds_read_tr16_b64_v4i16((LAS s4v*)(L + off))); }
DI v4u trd2(LAS char* L, u32 o0, u32 o1) { v2u a = trd(L, o0), b = trd(L, o1); v4u r = {a.x, a.y, b.x, b.y}; return r; }
template <int C> DI float dppadd(float v) { return v + __int_as_float(__builtin_amdgcn_update_dpp(0, __float_as_int(v), C, 0xF, 0xF, true)); }
DI float wsum(float v) {
  v = dppadd<0xB1>(v); v = dppadd<0x4E>(v); v = dppadd<0x141>(v); v = dppadd<0x140>(v);
  auto a = __builtin_amdgcn_permlane16_swap(__float_as_uint(v), __float_as_uint(v), false, false);
  v = __uint_as_float(a[0]) + __uint_as_float(a[1]);
  auto b = __builtin_amdgcn_permlane32_swap(__float_as_uint(v), __float_as_uint(v), false, false);
  return __uint_as_float(b[0]) + __uint_as_float(b[1]);
}
DI u64 mix64(u64 z) { z += 0x9E3779B97F4A7C15ull; z = (z ^ (z >> 30)) * 0xBF58476D1CE4E5B9ull; z = (z ^ (z >> 27)) * 0x94D049BB133111EBull; return z ^ (z >> 31); }
#define WAITL() asm volatile("s_waitcnt lgkmcnt(0)" ::: "memory")
template <int N> DI void waitv() { asm volatile("s_waitcnt vmcnt(%0)" ::"n"(N) : "memory"); }
#define SCHED() __builtin_amdgcn_sched_barrier(0)
DI u32 larrive(LAS char* L, int c, int lane) {
  WAITL();
  u32 old = 0;
  if (lane == 0) old = __hip_atomic_fetch_add((LAS u32*)(L + L_C) + c, 1u, __ATOMIC_RELAXED, __HIP_MEMORY_SCOPE_WORKGROUP);
  return __builtin_amdgcn_readfirstlane(old);
}
template <int SL = 0> DI void lwait(LAS char* L, int c, u32 n) {
  volatile LAS u32* p = (volatile LAS u32*)(L + L_C) + c;
  for (u32 s = 0; (u32)__builtin_amdgcn_readfirstlane(*p) < n && s < 40u * SPIN_CAP; ++s) { if (SL) __builtin_amdgcn_s_sleep(SL); }
  asm volatile("" ::: "memory");
}
DI void lsync(LAS char* L, int c, int lane) { larrive(L, c, lane); lwait(L, c, 4); }

#define STAMP(i)

template <int SGN> DI void dft8(const float (&ar)[8], const float (&ai)[8], float (&yr)[8], float (&yi)[8]) {
  const float R = 0.70710678118654752f, s = (float)SGN;
  float br[8], bi[8];
#pragma unroll
  for (int i = 0; i < 4; ++i) { br[i] = ar[i] + ar[i + 4]; bi[i] = ai[i] + ai[i + 4]; }
  br[4] = ar[0] - ar[4]; bi[4] = ai[0] - ai[4];
  { float r = ar[1] - ar[5], m = ai[1] - ai[5]; br[5] = R * (r - s * m); bi[5] = R * (m + s * r); }
  { float r = ar[2] - ar[6], m = ai[2] - ai[6]; br[6] = -s * m; bi[6] = s * r; }
  { float r = ar[3] - ar[7], m = ai[3] - ai[7]; br[7] = -R * (r + s * m); bi[7] = R * (s * r - m); }
#pragma unroll
  for (int h = 0; h < 2; ++h) {
    const int o = 4 * h;
    float er = br[o] + br[o + 2], ei = bi[o] + bi[o + 2], fr = br[o] - br[o + 2], fi = bi[o] - bi[o + 2];
    float gr = br[o + 1] + br[o + 3], gi = bi[o + 1] + bi[o + 3], hr = br[o + 1] - br[o + 3], hi = bi[o + 1] - bi[o + 3];
    yr[h] = er + gr; yi[h] = ei + gi; yr[h + 4] = er - gr; yi[h + 4] = ei - gi;
    yr[h + 2] = fr - s * hi; yi[h + 2] = fi + s * hr; yr[h + 6] = fr + s * hi; yi[h + 6] = fi - s * hr;
  }
}

struct P2C { v4u f1r, f1i, fr, fi, gr, gi, g1r, g1i; float tc[4], ts[4], uc[4], us[4]; };
DI v4u p2_column(const P2C& c, v4u in, v4u wv) {
  const f4v z = {0.f, 0.f, 0.f, 0.f};
  f4v r = mfma16(in, c.f1r, z), m = mfma16(in, c.f1i, z);
  v4u t;
#pragma unroll
  for (int i = 0; i < 4; ++i) t[i] = pk(r[i] * c.tc[i] + m[i] * c.ts[i], m[i] * c.tc[i] - r[i] * c.ts[i]);
  r = mfma16(c.fr, t, z); m = mfma16(c.fi, t, z);
#pragma unroll
  for (int i = 0; i < 4; ++i) { const float a = blo(wv[i]), b = bhi(wv[i]); t[i] = pk(r[i] * a - m[i] * b, r[i] * b + m[i] * a); }
  r = mfma16(t, c.gr, z); m = mfma16(t, c.gi, z);
#pragma unroll
  for (int i = 0; i < 4; ++i) t[i] = pk(r[i] * c.uc[i] - m[i] * c.us[i], m[i] * c.uc[i] + r[i] * c.us[i]);
  r = mfma16(c.g1r, t, z); m = mfma16(c.g1i, t, z);
#pragma unroll
  for (int i = 0; i < 4; ++i) t[i] = san(pk(r[i], m[i]));
  return t;
}

DI void dma16(u32 ldsaddr, u32 voff, v4i srd, u32 soff) {
  asm volatile("s_mov_b32 m0, %0\n\ts_nop 0\n\tbuffer_load_dwordx4 %1, %2, %3 offen lds" ::"s"(ldsaddr), "v"(voff), "s"(srd), "s"(soff) : "memory");
}
DI void ld16s(v4u& d, u32 voff, v4i srd, u32 soff) { asm volatile("buffer_load_dwordx4 %0, %1, %2, %3 offen sc1" : "=v"(d) : "v"(voff), "s"(srd), "s"(soff) : "memory"); }
DI void ld8s(v2u& d, u32 voff, v4i srd, u32 soff) { asm volatile("buffer_load_dwordx2 %0, %1, %2, %3 offen sc1" : "=v"(d) : "v"(voff), "s"(srd), "s"(soff) : "memory"); }
DI void ld8(v2u& d, u32 voff, v4i srd, u32 soff) { asm volatile("buffer_load_dwordx2 %0, %1, %2, %3 offen" : "=v"(d) : "v"(voff), "s"(srd), "s"(soff) : "memory"); }
DI void st16s(v4u d, u32 voff, v4i srd, u32 soff) { asm volatile("buffer_store_dwordx4 %0, %1, %2, %3 offen sc1\n\ts_nop 1" ::"v"(d), "v"(voff), "s"(srd), "s"(soff) : "memory"); }
DI void st8s(v2u d, u32 voff, v4i srd, u32 soff) { asm volatile("buffer_store_dwordx2 %0, %1, %2, %3 offen sc1\n\ts_nop 1" ::"v"(d), "v"(voff), "s"(srd), "s"(soff) : "memory"); }
template <int N> DI void waitv2(v4u& a, v4u& b) { asm volatile("s_waitcnt vmcnt(%2)" : "+v"(a), "+v"(b) : "n"(N) : "memory"); }
DI v4i mksrd(const void* p, u32 bytes) { const u64 a = (u64)p; v4i r = {(int)(u32)a, (int)((u32)(a >> 32) & 0xFFFFu), (int)bytes, 0x00020000}; return r; }

constexpr int RING = 6;
constexpr int g1_extra(int k) { return (k == 3 ? 8 : 0) + (k == 25 ? 4 : 0) + (k >= 26 && k <= 29 ? 4 : 0) + (k == 30 ? 4 : 0); }
constexpr int g1_after(int k) {
  int n = 0;
  if (k < RING) n += 2 * (RING - 1 - k);
  else n += g1_extra(k - RING);
  for (int j = (k < RING ? 0 : k - RING + 1); j < k; ++j) n += (j + RING < 32 ? 2 : 0) + g1_extra(j);
  return n;
}
constexpr int g2_extra(int k) { return k == 22 ? 3 : 0; }
constexpr int g2_after(int k) {
  int n = 0;
  if (k < RING) n += 2 * (RING - 1 - k);
  else n += g2_extra(k - RING);
  for (int j = (k < RING ? 0 : k - RING + 1); j < k; ++j) n += (j + RING < 28 ? 2 : 0) + g2_extra(j);
  return n;
}

template <int K> struct IC { static constexpr int v = K; };
template <int B, int E, class Fn> DI void sfor(Fn&& f) { if constexpr (B < E) { f(IC<B>{}); sfor<B + 1, E>(f); } }

__global__ void __launch_bounds__(512, 2) fused_kernel(Params p) {
  extern __shared__ __attribute__((aligned(16))) char smem[];
  LAS char* L = (LAS char*)smem;
  const int tid = threadIdx.x, lane = tid & 63, wv = __builtin_amdgcn_readfirstlane(tid >> 6), w = wv & 3;
  const int b = blockIdx.x, xl = b & 7, idx = b >> 3;
  const int dtl = idx & 3, dt = 4 * (xl >> 2) + dtl, grp = xl & 3;
  const int st = 8 * grp + (idx >> 2);
  STAMP(0);
  const rsrc_t rw = __builtin_amdgcn_make_buffer_rsrc((void*)p.ws, 0, (int)WS_BYTES, 0x00020000);
  asm volatile("" ::"s"(p.x), "s"(p.wr), "s"(p.wi), "s"(p.w1), "s"(p.b1), "s"(p.w2), "s"(p.b2), "s"(p.gw), "s"(p.out), "s"(p.ws));
  v4u fin_ga; u32 fin_om, fin_b2;

  if (wv < 4) {
    __builtin_amdgcn_s_setprio(3);
    const int s0 = b, q = lane >> 4, c = lane & 15;
    const rsrc_t rx = __builtin_amdgcn_make_buffer_rsrc((void*)p.x, 0, 2048 * 512 * 2, 0x00020000);
    const v4u x0 = __builtin_amdgcn_raw_buffer_load_b128(rx, lane * 16, (s0 + 512 * w) * 1024, 0);
    const v4u x1 = __builtin_amdgcn_raw_buffer_load_b128(rx, lane * 16, (s0 + 512 * w + 256) * 1024, 0);
    const v4u gwv = *(const v4u*)(p.gw + lane * 8);
    const v2u epv = __builtin_amdgcn_raw_buffer_load_b64(rw, O_EPOCH, 0, 0);
    {
      const v4u P = {POISON, POISON, POISON, POISON};
      __builtin_amdgcn_raw_buffer_store_b128(P, rw, lane * 16, O_X1 + (2 * w) * 262144 + s0 * 1024, AUX_SC1);
      __builtin_amdgcn_raw_buffer_store_b128(P, rw, lane * 16, O_X1 + (2 * w + 1) * 262144 + s0 * 1024, AUX_SC1);
      __builtin_amdgcn_raw_buffer_store_b128(P, rw, lane * 16, O_V + b * 8192 + w * 2048, AUX_SC1);
      __builtin_amdgcn_raw_buffer_store_b128(P, rw, lane * 16, O_V + b * 8192 + w * 2048 + 1024, AUX_SC1);
      __builtin_amdgcn_raw_buffer_store_b128(P, rw, lane * 16, O_GA + b * 8192 + w * 2048, AUX_SC1);
      __builtin_amdgcn_raw_buffer_store_b128(P, rw, lane * 16, O_GA + b * 8192 + w * 2048 + 1024, AUX_SC1);
      if (w == 0 && lane < 24)
        __builtin_amdgcn_raw_buffer_store_b32(POISON, rw, lane < 8 ? O_XN + lane * 1024 + s0 * 4 : (lane < 16 ? O_VN : O_OMG) + b * 32 + (lane & 7) * 4, 0, AUX_SC1);
    }
    if (tid < C_N) ((LAS u32*)(L + L_C))[tid] = 0;
    WAITL();
    __builtin_amdgcn_s_barrier();
    float xa[8], xb[8];
#pragma unroll
    for (int i = 0; i < 4; ++i) { xa[2 * i] = blo(x0[i]); xa[2 * i + 1] = bhi(x0[i]); xb[2 * i] = blo(x1[i]); xb[2 * i + 1] = bhi(x1[i]); }
    STAMP(1);
    *(LAS v4u*)(L + L_X + (2 * w) * 1088 + lane * 16) = x0;
    *(LAS v4u*)(L + L_X + (2 * w + 1) * 1088 + lane * 16) = x1;
    {
      float sa = ((xa[0] + xa[1]) + (xa[2] + xa[3])) + ((xa[4] + xa[5]) + (xa[6] + xa[7]));
      float sb = ((xb[0] + xb[1]) + (xb[2] + xb[3])) + ((xb[4] + xb[5]) + (xb[6] + xb[7]));
      const float ma = wsum(sa) * (1.f / 512.f), mb = wsum(sb) * (1.f / 512.f);
      float va = 0.f, vb = 0.f;
#pragma unroll
      for (int i = 0; i < 8; ++i) { const float da = xa[i] - ma, db = xb[i] - mb; va += da * da; vb += db * db; }
      va = wsum(va) * (1.f / 512.f); vb = wsum(vb) * (1.f / 512.f);
      const float ra = __builtin_amdgcn_rcpf(rbf(__builtin_amdgcn_sqrtf(rbf(rbf(va) + 1e-5f))));
      const float rb = __builtin_amdgcn_rcpf(rbf(__builtin_amdgcn_sqrtf(rbf(rbf(vb) + 1e-5f))));
      const float na = -rbf(ma) * ra, nb = -rbf(mb) * rb;
      v4u ha, hb;
#pragma unroll
      for (int i = 0; i < 4; ++i) {
        ha[i] = pk(xa[2 * i] * ra + na, xa[2 * i + 1] * ra + na);
        hb[i] = pk(xb[2 * i] * rb + nb, xb[2 * i + 1] * rb + nb);
      }
      *(LAS v4u*)(L + L_HR + (2 * w) * 1088 + lane * 16) = ha;
      *(LAS v4u*)(L + L_HR + (2 * w + 1) * 1088 + lane * 16) = hb;
    }
    lsync(L, C_H1, lane);
    {
      const u32 ta = L_HR + (c >> 2) * 1088 + (128 * w + 16 * q + 4 * (c & 3)) * 2;
      const v4u h0 = trd2(L, ta, ta + 4 * 1088), h1 = trd2(L, ta + 128, ta + 128 + 4 * 1088);
      const u32 vo = (lane >> 5) * 131072 + (lane & 31) * 16;
      __builtin_amdgcn_raw_buffer_store_b128(h0, rw, vo, O_H + (4 * w) * 131072 + s0 * 512, AUX_SC1);
      __builtin_amdgcn_raw_buffer_store_b128(h1, rw, vo, O_H + (4 * w + 2) * 131072 + s0 * 512, AUX_SC1);
    }
    STAMP(2);
    float om_keep;
    {
      float ga = 0.f, gb = 0.f;
#pragma unroll
      for (int i = 0; i < 4; ++i) {
        const float g0 = blo(gwv[i]), g1 = bhi(gwv[i]);
        const u32 pa = pk(xa[2 * i] * g0, xa[2 * i + 1] * g1), pb = pk(xb[2 * i] * g0, xb[2 * i + 1] * g1);
        ga += blo(pa) + bhi(pa); gb += blo(pb) + bhi(pb);
      }
      ga = rbf(wsum(ga)); gb = rbf(wsum(gb));
      const float gs = (lane & 1) ? gb : ga;
      const float g = rbf(__builtin_amdgcn_rcpf(1.f + __builtin_amdgcn_exp2f(-1.44269504088896f * gs)));
      om_keep = rbf(1.f - g);
      if (lane < 2) ((LAS float*)(L + L_C))[C_G + 2 * w + lane] = g;
    }
    v4u f64r[2], f64i[2], a8;
    float twc[4], tws[4];
    {
      const int k1 = 16 * w + c;
#pragma unroll
      for (int ks = 0; ks < 2; ++ks)
#pragma unroll
        for (int j = 0; j < 4; ++j) {
          const int a = 32 * ks + 8 * q + 2 * j, n0 = (a * k1) & 63, n1 = ((a + 1) * k1) & 63;
          f64r[ks][j] = pk(cosr(n0, 1.f / 64), cosr(n1, 1.f / 64));
          f64i[ks][j] = pk(-sinr(n0, 1.f / 64), -sinr(n1, 1.f / 64));
        }
#pragma unroll
      for (int r = 0; r < 4; ++r) {
        const int bb = 4 * (q & 1) + r, n = (bb * (c >> 2)) & 7;
        const float cs = cosr(n, 0.125f), sn = sinr(n, 0.125f);
        const u32 e = (c & 1) ? pk(-sn, cs) : pk(cs, sn);
        a8[r] = (((c >> 1) & 1) == (q >> 1)) ? e : 0u;
        twc[r] = cosr(bb * k1, 1.f / 512); tws[r] = sinr(bb * k1, 1.f / 512);
      }
    }
    const u64 tag = mix64(((u64)(u32)__builtin_amdgcn_readfirstlane(epv.y) << 32) | (u32)__builtin_amdgcn_readfirstlane(epv.x));
    if (lane == 0) { const v2u tg = {(u32)tag, (u32)(tag >> 32)}; *(LAS v2u*)(L + L_C + C_TAG * 4) = tg; }
    waitv<0>();
    STAMP(3);
    if (larrive(L, C_F1, lane) == 3 && lane < 8) {
      const v2u tg = {(u32)tag, (u32)(tag >> 32)};
      __builtin_amdgcn_raw_buffer_store_b64(tg, rw, O_F1 + lane * 2048 + b * 8, 0, AUX_SC1);
    }
    const int j1 = xl, kb = idx;
    v4u wq0, wq1;
    {
      const rsrc_t rr = __builtin_amdgcn_make_buffer_rsrc((void*)p.wr, 0, 2048 * 257 * 2, 0x00020000);
      const rsrc_t ri = __builtin_amdgcn_make_buffer_rsrc((void*)p.wi, 0, 2048 * 257 * 2, 0x00020000);
      const u32 o = (u32)(j1 + 8 * (64 * w + lane)) * 514u + kb * 16;
      wq0 = __builtin_amdgcn_raw_buffer_load_b128(rr, o, 0, 0);
      wq1 = __builtin_amdgcn_raw_buffer_load_b128(ri, o, 0, 0);
    }
    {
      float yr[8], yi[8], nr[8], ni[8];
      const f4v z = {0.f, 0.f, 0.f, 0.f};
      const u32 xo = L_X + ((c >> 1) & 1) * 1088 + (8 * q + (c >> 2)) * 16 + 8 * (c & 1);
      v4u a8n = a8; a8n[1] ^= 0x80008000u; a8n[3] ^= 0x80008000u;
#pragma unroll
      for (int pi = 0; pi < 4; ++pi) {
        const v4u A0 = trd2(L, xo + pi * 2176, xo + pi * 2176 + 64), A1 = trd2(L, xo + pi * 2176 + 512, xo + pi * 2176 + 576);
        f4v r = mfma16(A0, f64r[0], z), m = mfma16(A0, f64i[0], z);
        r = mfma16(A1, f64r[1], r); m = mfma16(A1, f64i[1], m);
        v4u t;
#pragma unroll
        for (int i = 0; i < 4; ++i) t[i] = pk(r[i] * twc[i] + m[i] * tws[i], m[i] * twc[i] - r[i] * tws[i]);
        const f4v d = mfma16(a8, t, z);
        yr[2 * pi] = d[0]; yi[2 * pi] = d[1]; yr[2 * pi + 1] = d[2]; yi[2 * pi + 1] = d[3];
        if (w == 0) { const f4v e = mfma16(a8n, t, z); nr[2 * pi] = e[0]; ni[2 * pi] = e[1]; nr[2 * pi + 1] = e[2]; ni[2 * pi + 1] = e[3]; }
      }
      float zr[8], zi[8];
      dft8<-1>(yr, yi, zr, zi);
#pragma unroll
      for (int j = 0; j < 8; ++j) *(LAS u32*)(L + L_T + w * 2048 + j * 256 + lane * 4) = san(pk(zr[j], zi[j]));
      const v4u o0 = *(LAS v4u*)(L + L_T + w * 2048 + lane * 16), o1 = *(LAS v4u*)(L + L_T + w * 2048 + 1024 + lane * 16);
      lwait(L, C_F1, 4);
      const u32 vo = q * 262144 + (((lane >> 2) & 3) * 64 + (lane & 3) * 4) * 4;
      __builtin_amdgcn_raw_buffer_store_b128(o0, rw, vo, O_X1 + s0 * 1024 + w * 64, AUX_SC1);
      __builtin_amdgcn_raw_buffer_store_b128(o1, rw, vo, O_X1 + 4 * 262144 + s0 * 1024 + w * 64, AUX_SC1);
      if (w == 0) {
        dft8<-1>(nr, ni, zr, zi);
        if (lane == 0) {
#pragma unroll
          for (int j = 0; j < 8; ++j) __builtin_amdgcn_raw_buffer_store_b32(san(pk(zr[j], zi[j])), rw, O_XN + j * 1024 + s0 * 4, 0, AUX_SC1);
        }
      }
    }
    STAMP(4);
    const u32 epos = ((w * 16 + c) * 4 + q) * 4;
    P2C pc;
    {
#pragma unroll
      for (int i = 0; i < 4; ++i) {
        const int e = 4 * q + i;
        { const int n = (e * (8 * c + j1)) & 127; const float cs = cosr(n, 1.f / 128), sn = sinr(n, 1.f / 128); pc.f1r[i] = pk(cs, sn); pc.f1i[i] = pk(-sn, cs); }
        { const int n = (e * c) & 15; const float cs = cosr(n, 1.f / 16), sn = sinr(n, 1.f / 16); pc.fr[i] = pk(cs, sn); pc.fi[i] = pk(-sn, cs); pc.gr[i] = pk(cs, -sn); pc.gi[i] = pk(sn, cs); }
        { const int n = (c * (8 * e + j1)) & 127; const float cs = cosr(n, 1.f / 128), sn = sinr(n, 1.f / 128); pc.g1r[i] = pk(cs, -sn); pc.g1i[i] = pk(sn, cs); }
        pc.tc[i] = cosr(e * (8 * c + j1), 1.f / 2048); pc.ts[i] = sinr(e * (8 * c + j1), 1.f / 2048);
        pc.uc[i] = cosr(c * (8 * e + j1), 1.f / 2048); pc.us[i] = sinr(c * (8 * e + j1), 1.f / 2048);
      }
#pragma unroll
      for (int i = 0; i < 4; ++i) {
        u32 lo = __builtin_amdgcn_perm(wq1[i], wq0[i], 0x05040100u), hi = __builtin_amdgcn_perm(wq1[i], wq0[i], 0x07060302u);
        if (i == 0 && kb == 0) lo = pk(0.5f * blo(lo), 0.5f * bhi(lo));
        *(LAS u32*)(L + L_W + (2 * i) * 1024 + epos) = lo;
        *(LAS u32*)(L + L_W + (2 * i + 1) * 1024 + epos) = hi;
      }
    }
    {
      const u32 fo = O_F1 + xl * 2048 + (64 * w + lane) * 8;
      for (u32 s = 0; s < SPIN_CAP; ++s) {
        const v2u f = __builtin_amdgcn_raw_buffer_load_b64(rw, fo, 0, AUX_SC1);
        if (__all(f.x == (u32)tag && f.y == (u32)(tag >> 32))) break;
        __builtin_amdgcn_s_sleep(2);
      }
      asm volatile("" ::: "memory");
      if (lane == 0) { ((volatile LAS u32*)(L + L_C))[C_OK + w] = 1u; }
      STAMP(5);
      lwait(L, C_OK + 0, 1); lwait(L, C_OK + 1, 1); lwait(L, C_OK + 2, 1); lwait(L, C_OK + 3, 1);
    }
    if (b == 0 && tid == 0) *(u64*)(p.ws + O_EPOCH) = tag;
    if (lane < 2) __builtin_amdgcn_raw_buffer_store_b32(__float_as_uint(om_keep), rw, O_OMG + (s0 + 256 * (2 * w + lane)) * 4, 0, AUX_SC1);
    {
      const u32 xo = O_X1 + j1 * 262144 + (64 * w + lane) * 1024 + kb * 32;
      v4u e0 = __builtin_amdgcn_raw_buffer_load_b128(rw, xo, 0, AUX_SC1), e1 = __builtin_amdgcn_raw_buffer_load_b128(rw, xo + 16, 0, AUX_SC1);
      for (u32 s = 0; s < SPIN_CAP; ++s) {
        const bool bad = max(max4(e0), max4(e1)) == POISON;
        if (!__any(bad)) break;
        __builtin_amdgcn_s_sleep(1);
        if (bad) { e0 = __builtin_amdgcn_raw_buffer_load_b128(rw, xo, 0, AUX_SC1); e1 = __builtin_amdgcn_raw_buffer_load_b128(rw, xo + 16, 0, AUX_SC1); }
      }
      STAMP(6);
#pragma unroll
      for (int i = 0; i < 4; ++i) { *(LAS u32*)(L + L_E + i * 1024 + epos) = e0[i]; *(LAS u32*)(L + L_E + (4 + i) * 1024 + epos) = e1[i]; }
      lsync(L, C_P2A, lane);
      const v4u i0 = *(LAS v4u*)(L + L_E + (2 * w) * 1024 + lane * 16), i1 = *(LAS v4u*)(L + L_E + (2 * w + 1) * 1024 + lane * 16);
      const v4u w0 = *(LAS v4u*)(L + L_W + (2 * w) * 1024 + lane * 16), w1 = *(LAS v4u*)(L + L_W + (2 * w + 1) * 1024 + lane * 16);
      *(LAS v4u*)(L + L_O + (2 * w) * 1024 + lane * 16) = p2_column(pc, i0, w0);
      *(LAS v4u*)(L + L_O + (2 * w + 1) * 1024 + lane * 16) = p2_column(pc, i1, w1);
      lsync(L, C_P2B, lane);
      v4u o0, o1;
#pragma unroll
      for (int i = 0; i < 4; ++i) { o0[i] = *(LAS u32*)(L + L_O + i * 1024 + epos); o1[i] = *(LAS u32*)(L + L_O + (4 + i) * 1024 + epos); }
      const u32 vo = O_V + (64 * w + lane) * 8192 + j1 * 1024 + kb * 32;
      __builtin_amdgcn_raw_buffer_store_b128(o0, rw, vo, 0, AUX_SC1);
      __builtin_amdgcn_raw_buffer_store_b128(o1, rw, vo + 16, 0, AUX_SC1);
      if (kb == 31 && w == 0) {
        v4u in, wn;
#pragma unroll
        for (int i = 0; i < 4; ++i) {
          const u32 e = 64 * q + 16 * i + c, o = (j1 + 8 * e) * 257 + 256;
          wn[i] = pk(0.5f * blo(p.wr[o]), 0.5f * blo(p.wi[o]));
          in[i] = POISON;
        }
        for (u32 s = 0; s < SPIN_CAP; ++s) {
#pragma unroll
          for (int i = 0; i < 4; ++i) in[i] = __builtin_amdgcn_raw_buffer_load_b32(rw, O_XN + j1 * 1024 + (64 * q + 16 * i + c) * 4, 0, AUX_SC1);
          if (!__any(max4(in) == POISON)) break;
        }
        const v4u o = p2_column(pc, in, wn);
#pragma unroll
        for (int i = 0; i < 4; ++i) __builtin_amdgcn_raw_buffer_store_b32(o[i], rw, O_VN + (64 * q + 16 * i + c) * 32 + j1 * 4, 0, AUX_SC1);
      }
    }
    STAMP(7);
    {
      v4u gB[4];
      float pcs[2], psn[2];
      u32 ax;
      {
        const int ap = 16 * w + c;
#pragma unroll
        for (int ks = 0; ks < 4; ++ks)
#pragma unroll
          for (int i = 0; i < 4; ++i) {
            const int n = ((16 * ks + 4 * q + i) * ap) & 63;
            gB[ks][i] = pk(cosr(n, 1.f / 64) * 0x1p-19f, -sinr(n, 1.f / 64) * 0x1p-19f);
          }
        const int n = (q * (c >> 1)) & 7;
        const float cs = cosr(n, 0.125f), sn = sinr(n, 0.125f);
        ax = (c & 1) ? pk(sn, cs) : pk(cs, -sn);
#pragma unroll
        for (int e = 0; e < 2; ++e) { pcs[e] = cosr(ap * (2 * q + e), 1.f / 512); psn[e] = sinr(ap * (2 * q + e), 1.f / 512); }
      }
      const u32 vo = O_V + s0 * 8192 + (16 * w + 64 * q + c) * 4;
      u32 vv[8];
#pragma unroll
      for (int j = 0; j < 8; ++j) vv[j] = __builtin_amdgcn_raw_buffer_load_b32(rw, vo + j * 1024, 0, AUX_SC1);
      for (u32 s = 0; s < SPIN_CAP; ++s) {
        const bool bad = max(max(max(vv[0], vv[1]), max(vv[2], vv[3])), max(max(vv[4], vv[5]), max(vv[6], vv[7]))) == POISON;
        if (!__any(bad)) break;
        __builtin_amdgcn_s_sleep(1);
        if (bad) {
#pragma unroll
          for (int j = 0; j < 8; ++j) vv[j] = __builtin_amdgcn_raw_buffer_load_b32(rw, vo + j * 1024, 0, AUX_SC1);
        }
      }
      STAMP(8);
      float ar[8], ai[8], yr[8], yi[8];
#pragma unroll
      for (int j = 0; j < 8; ++j) { ar[j] = blo(vv[j]); ai[j] = bhi(vv[j]); }
      dft8<1>(ar, ai, yr, yi);
      v4u yq[2];
#pragma unroll
      for (int i = 0; i < 8; ++i) yq[i >> 2][i & 3] = pk(yr[i], yi[i]);
      const f4v z = {0.f, 0.f, 0.f, 0.f};
#pragma unroll
      for (int i = 0; i < 8; ++i) {
        v4u A = {0u, 0u, 0u, 0u};
        A[i & 3] = ax;
        const f4v d = mfma16(A, yq[i >> 2], z);
        *(LAS u32*)(L + L_U + i * 2176 + (2 * q) * 272 + (16 * w + c) * 4) = pk(d[0] * pcs[0] - d[1] * psn[0], d[1] * pcs[0] + d[0] * psn[0]);
        *(LAS u32*)(L + L_U + i * 2176 + (2 * q + 1) * 272 + (16 * w + c) * 4) = pk(d[2] * pcs[1] - d[3] * psn[1], d[3] * pcs[1] + d[2] * psn[1]);
      }
      if (w == 0) {
        v4u n0, n1;
        for (u32 s = 0; s < SPIN_CAP; ++s) {
          n0 = __builtin_amdgcn_raw_buffer_load_b128(rw, O_VN + s0 * 32, 0, AUX_SC1);
          n1 = __builtin_amdgcn_raw_buffer_load_b128(rw, O_VN + s0 * 32 + 16, 0, AUX_SC1);
          if (!__any(max(max4(n0), max4(n1)) == POISON)) break;
        }
#pragma unroll
        for (int j = 0; j < 4; ++j) { ar[j] = blo(n0[j]); ai[j] = bhi(n0[j]); ar[j + 4] = blo(n1[j]); ai[j + 4] = bhi(n1[j]); }
        dft8<1>(ar, ai, yr, yi);
        if (lane == 0) {
#pragma unroll
          for (int i = 0; i < 8; ++i) ((LAS float*)(L + L_C))[C_NY + i] = yr[i] * 0x1p-19f;
        }
      }
      lsync(L, C_P3B, lane);
      const u32 uo = L_U + (c >> 3) * 2176 + (c & 7) * 272 + q * 16;
#pragma unroll
      for (int pi = 0; pi < 4; ++pi) {
        f4v d = z;
#pragma unroll
        for (int ks = 0; ks < 4; ++ks) d = mfma16(*(LAS v4u*)(L + uo + pi * 4352 + ks * 64), gB[ks], d);
        const int i = 2 * pi + (q >> 1);
        const float ny = ((LAS float*)(L + L_C))[C_NY + i], g = ((LAS float*)(L + L_C))[C_G + i];
        const u32 a01 = pk(d[0] + ny, d[1] - ny), a23 = pk(d[2] + ny, d[3] - ny);
        const v2u o = {san(pk(g * blo(a01), g * bhi(a01))), san(pk(g * blo(a23), g * bhi(a23)))};
        __builtin_amdgcn_raw_buffer_store_b64(o, rw, O_GA + (s0 + 256 * i) * 1024 + ((16 * w + c) * 8 + 4 * (q & 1)) * 2, 0, AUX_SC1);
      }
    }
    STAMP(9);
    {
      const int sl = (tid >> 3) & 63, s = 64 * st + sl;
      fin_b2 = p.b2[s];
      const u32 go = O_GA + s * 1024 + dt * 128 + (tid & 7) * 16;
      for (u32 sp = 0; sp < SPIN_CAP; ++sp) {
        fin_ga = __builtin_amdgcn_raw_buffer_load_b128(rw, go, 0, AUX_SC1);
        fin_om = __builtin_amdgcn_raw_buffer_load_b32(rw, O_OMG + s * 4, 0, AUX_SC1);
        if (!__any(max(max4(fin_ga), fin_om) == POISON)) break;
        __builtin_amdgcn_s_sleep(4);
      }
    }
    STAMP(10);
  } else {
    __builtin_amdgcn_s_barrier();
    const v4i sw = mksrd(p.ws, WS_BYTES), s2 = mksrd(p.w2, 1792u * 4096u);
    u32 lb = (u32)(size_t)smem + L_G + w * 32768, w2b = (u32)(448 * w) * 4096u + st * 128, rot2 = 7 * dtl;
    const u32 lo = L_G + w * 32768;
    const int hh = lane >> 5, blk = (lane >> 4) & 1, tq = (lane & 15) >> 2, tp = lane & 3;
    const u32 v2off = (lane >> 3) * 4096 + (((lane & 7) ^ (((lane >> 4) & 1) << 2)) * 16);
    auto w2dma = [&](int kl, u32 dst) {
      u32 kg = kl + rot2; kg = kg >= 28 ? kg - 28 : kg;
      const u32 so = w2b + kg * 65536u;
      dma16(lb + dst, v2off, s2, so);
      dma16(lb + dst + 1024, v2off, s2, so + 8 * 4096);
    };
    const u32 t2 = lo + (4 * hh + tq) * 128 + (((2 * blk + (tp >> 1)) ^ (((tq >> 1) & 1) << 2)) * 16) + 8 * (tp & 1);
    v4u wr_[24];
    f16v acc[4];
    v4u ring[2 * RING];
    if (idx < 28) {
      const int ft = 7 * grp + (idx >> 2);
      const v4i s1 = mksrd(p.w1, 2048u * 3584u);
      u32 v1off[4];
#pragma unroll
      for (int j = 0; j < 4; ++j) v1off[j] = ((lane >> 3) ^ (j >> 1)) * 3584 + ((lane & 7) ^ ((j & 1) << 2)) * 16;
      auto w1dma = [&](int bl, int slot) {
        const u32 so = (u32)(64 * w + 8 * ((bl + 2 * dtl) & 7)) * 3584u + ft * 128;
#pragma unroll
        for (int j = 0; j < 8; ++j) dma16(lb + slot * 8192 + j * 1024, v1off[j & 3], s1, so + j * (256u * 3584u));
      };
      const u32 t1 = lo + tq * 1024 + ((hh ^ (tq >> 1)) * 128) + ((((w >> 1) * 4 + 2 * blk + (tp >> 1)) ^ ((tq & 1) << 2)) * 16) + 8 * (tp & 1);
      const u32 t1x = t1 ^ 64u;
      w1dma(0, 0); w1dma(1, 1); w1dma(2, 2); w1dma(3, 3);
      STAMP(1);
      sfor<0, 3>([&](auto B) {
        constexpr int bl = decltype(B)::v;
        waitv<24>();
#pragma unroll
        for (int k = 0; k < 4; ++k) {
          wr_[8 * bl + 2 * k] = trd2(L, t1 + bl * 8192 + k * 256, t1 + bl * 8192 + k * 256 + 4096);
          wr_[8 * bl + 2 * k + 1] = trd2(L, t1x + bl * 8192 + k * 256, t1x + bl * 8192 + k * 256 + 4096);
        }
        WAITL();
        w1dma(4 + bl, bl);
      });
      STAMP(2);
      lwait<1>(L, C_OK + w, 1);
      STAMP(3);
      auto hload = [&](int k) {
        const int pos = k >> 2, bl = pos < 4 ? pos + 3 : (pos < 7 ? pos - 4 : 7);
        const u32 so = O_H + (u32)(64 * w + 8 * ((bl + 2 * dtl) & 7) + 2 * (k & 3)) * 512u;
        ld16s(ring[2 * (k % RING)], lane * 16, sw, so + (2 * dt + (w & 1)) * 131072);
        ld16s(ring[2 * (k % RING) + 1], lane * 16, sw, so + (2 * dt + ((w & 1) ^ 1)) * 131072);
      };
#pragma unroll
      for (int k = 0; k < RING; ++k) hload(k);
#pragma unroll
      for (int i = 0; i < 4; ++i)
#pragma unroll
        for (int j = 0; j < 16; ++j) acc[i][j] = 0.f;
      v2u bias[4];
      v4u wf[2][2];
      sfor<0, 32>([&](auto K) {
        constexpr int k = decltype(K)::v, pos = k >> 2;
        waitv2<g1_after(k)>(ring[2 * (k % RING)], ring[2 * (k % RING) + 1]);
        if constexpr (k == 0) {
          STAMP(4);
          wf[0][0] = trd2(L, t1 + 3 * 8192, t1 + 3 * 8192 + 4096); wf[0][1] = trd2(L, t1x + 3 * 8192, t1x + 3 * 8192 + 4096);
        }
        constexpr int kn = k + 1, pn = kn >> 2;
        if constexpr (kn < 32 && (pn < 4 || pn == 7)) {
          constexpr u32 o = (pn == 0 || pn == 7 ? 3 : pn - 1) * 8192 + (kn & 3) * 256;
          wf[kn & 1][0] = trd2(L, t1 + o, t1 + o + 4096); wf[kn & 1][1] = trd2(L, t1x + o, t1x + o + 4096);
        }
        const v4u a0 = (pos >= 4 && pos < 7) ? wr_[2 * ((k - 16) & 15) % 24] : wf[k & 1][0];
        const v4u a1 = (pos >= 4 && pos < 7) ? wr_[(2 * ((k - 16) & 15) + 1) % 24] : wf[k & 1][1];
        acc[0] = mfma32(a0, ring[2 * (k % RING)], acc[0]);
        acc[1] = mfma32(a0, ring[2 * (k % RING) + 1], acc[1]);
        acc[2] = mfma32(a1, ring[2 * (k % RING)], acc[2]);
        acc[3] = mfma32(a1, ring[2 * (k % RING) + 1], acc[3]);
        SCHED();
        if constexpr (k + RING < 32) hload(k + RING);
        if constexpr (k == 3) { WAITL(); w1dma(7, 3); }
        if constexpr (k == 25) {
          const v4i sb = mksrd(p.b1, 1792 * 2);
#pragma unroll
          for (int g = 0; g < 4; ++g) ld8(bias[g], (8 * g + 4 * hh) * 2, sb, (64 * ft + 32 * (w >> 1)) * 2);
        }
        if constexpr (k >= 26 && k <= 30) { w2dma(2 * (k - 26), 2 * (k - 26) * 2048); w2dma(2 * (k - 26) + 1, (2 * (k - 26) + 1) * 2048); }
        SCHED();
      });
      STAMP(5);
#pragma unroll
      for (int l = 1; l < 4; ++l)
#pragma unroll
        for (int g = 0; g < 4; ++g) {
          const f4v v = {acc[l][4 * g], acc[l][4 * g + 1], acc[l][4 * g + 2], acc[l][4 * g + 3]};
          *(LAS f4v*)(L + lo + 20480 + (l - 1) * 4096 + g * 1024 + lane * 16) = v;
        }
      lsync(L, C_R1, lane);
#pragma unroll
      for (int l = 1; l < 4; ++l)
#pragma unroll
        for (int g = 0; g < 4; ++g) {
          const f4v v = *(LAS f4v*)(L + L_G + (w ^ l) * 32768 + 20480 + (l - 1) * 4096 + g * 1024 + lane * 16);
#pragma unroll
          for (int e = 0; e < 4; ++e) acc[0][4 * g + e] += v[e];
        }
      larrive(L, C_R2, lane);
      asm volatile("s_waitcnt vmcnt(20)" : "+v"(bias[0]), "+v"(bias[1]), "+v"(bias[2]), "+v"(bias[3])::"memory");
      v4u uf[2];
#pragma unroll
      for (int g = 0; g < 4; ++g) {
        float o[4];
#pragma unroll
        for (int e = 0; e < 4; ++e) {
          const float v = acc[0][4 * g + e] + ((e & 1) ? bhi(bias[g][e >> 1]) : blo(bias[g][e >> 1]));
          const float t = v * (-2.3022081985f - 0.1029432397f * v * v);
          o[e] = v * __builtin_amdgcn_rcpf(1.f + __builtin_amdgcn_exp2f(t));
        }
        uf[g >> 1][2 * (g & 1)] = pk(o[0], o[1]); uf[g >> 1][2 * (g & 1) + 1] = pk(o[2], o[3]);
      }
      const u32 uo = O_U + (u32)((2 * dt + (w & 1)) * 112 + 4 * ft + 2 * (w >> 1)) * 1024u;
      st16s(uf[0], lane * 16, sw, uo);
      st16s(uf[1], lane * 16, sw, uo + 1024);
      STAMP(6);
      waitv<2>();
#pragma unroll
      for (int k = 0; k < 10; ++k) { wr_[2 * k] = trd2(L, t2 + k * 2048, t2 + k * 2048 + 1024); wr_[2 * k + 1] = trd2(L, (t2 ^ 64u) + k * 2048, (t2 ^ 64u) + k * 2048 + 1024); }
      WAITL();
#pragma unroll
      for (int k = 16; k < 20; ++k) w2dma(k, (k - 16) * 2048);
      waitv<8>();
      if (larrive(L, C_F2, lane) == 3 && lane == 0) st8s(*(LAS v2u*)(L + L_C + C_TAG * 4), 0, sw, O_F2 + (dt * 28 + ft) * 128);
      STAMP(7);
      lwait(L, C_R2, 4);
#pragma unroll
      for (int k = 10; k < 16; ++k) w2dma(k, k * 2048);
#pragma unroll
      for (int k = 20; k < 26; ++k) w2dma(k, (k - 16) * 2048);
      waitv<20>();
    } else {
#pragma unroll
      for (int k = 0; k < 16; ++k) w2dma(k, k * 2048);
      waitv<12>();
#pragma unroll
      for (int k = 0; k < 10; ++k) { wr_[2 * k] = trd2(L, t2 + k * 2048, t2 + k * 2048 + 1024); wr_[2 * k + 1] = trd2(L, (t2 ^ 64u) + k * 2048, (t2 ^ 64u) + k * 2048 + 1024); }
      WAITL();
#pragma unroll
      for (int k = 16; k < 26; ++k) w2dma(k, (k - 16) * 2048);
      waitv<20>();
    }
    asm volatile("" : "+s"(lb), "+s"(w2b), "+s"(rot2));
#pragma unroll
    for (int k = 10; k < 12; ++k) { wr_[2 * k] = trd2(L, t2 + k * 2048, t2 + k * 2048 + 1024); wr_[2 * k + 1] = trd2(L, (t2 ^ 64u) + k * 2048, (t2 ^ 64u) + k * 2048 + 1024); }
    WAITL();
    w2dma(26, 10 * 2048); w2dma(27, 11 * 2048);
    lwait<1>(L, C_OK + 0, 1); lwait<1>(L, C_OK + 1, 1); lwait<1>(L, C_OK + 2, 1); lwait<1>(L, C_OK + 3, 1);
    STAMP(8);
    {
      const u32 fo = O_F2 + (dt * 28 + 7 * w + (lane < 7 ? lane : 6)) * 128;
      const v2u tg = *(LAS v2u*)(L + L_C + C_TAG * 4);
      for (u32 s = 0; s < SPIN_CAP; ++s) {
        v2u f;
        ld8s(f, fo, sw, 0);
        asm volatile("s_waitcnt vmcnt(0)" : "+v"(f)::"memory");
        if (__all(f.x == tg.x && f.y == tg.y)) break;
        __builtin_amdgcn_s_sleep(1);
      }
    }
    STAMP(9);
    auto uload = [&](int k) {
      u32 kg = k + rot2; kg = kg >= 28 ? kg - 28 : kg;
      const u32 so = O_U + (u32)(2 * dt * 112 + 28 * w + kg) * 1024u;
      ld16s(ring[2 * (k % RING)], lane * 16, sw, so);
      ld16s(ring[2 * (k % RING) + 1], lane * 16, sw, so + 112 * 1024);
    };
#pragma unroll
    for (int k = 0; k < RING; ++k) uload(k);
#pragma unroll
    for (int i = 0; i < 4; ++i)
#pragma unroll
      for (int j = 0; j < 16; ++j) acc[i][j] = 0.f;
    const int fs = 64 * st + ((tid >> 3) & 63);
    const u32 go = O_GA + fs * 1024 + dt * 128 + (tid & 7) * 16;
    {
      v4u wf[2][2];
      sfor<0, 28>([&](auto K) {
        constexpr int k = decltype(K)::v;
        waitv2<g2_after(k)>(ring[2 * (k % RING)], ring[2 * (k % RING) + 1]);
        if constexpr (k == 0) { STAMP(10); }
        constexpr int kn = k + 1;
        if constexpr (kn >= 12 && kn < 28) {
          constexpr u32 o = (kn < 16 ? kn : kn - 16) * 2048;
          wf[kn & 1][0] = trd2(L, t2 + o, t2 + o + 1024); wf[kn & 1][1] = trd2(L, (t2 ^ 64u) + o, (t2 ^ 64u) + o + 1024);
        }
        const v4u b0 = k < 12 ? wr_[(2 * k) % 24] : wf[k & 1][0];
        const v4u b1 = k < 12 ? wr_[(2 * k + 1) % 24] : wf[k & 1][1];
        acc[0] = mfma32(ring[2 * (k % RING)], b0, acc[0]);
        acc[1] = mfma32(ring[2 * (k % RING)], b1, acc[1]);
        acc[2] = mfma32(ring[2 * (k % RING) + 1], b0, acc[2]);
        acc[3] = mfma32(ring[2 * (k % RING) + 1], b1, acc[3]);
        SCHED();
        if constexpr (k + RING < 28) uload(k + RING);
        if constexpr (k == 22) {
          ld16s(fin_ga, go, sw, 0);
          asm volatile("buffer_load_dword %0, %1, %2, 0 offen sc1" : "=v"(fin_om) : "v"(O_OMG + fs * 4), "s"(sw) : "memory");
          const v4i sb = mksrd(p.b2, 2048 * 2);
          asm volatile("buffer_load_ushort %0, %1, %2, 0 offen" : "=v"(fin_b2) : "v"(fs * 2), "s"(sb) : "memory");
        }
        SCHED();
      });
    }
    STAMP(11);
#pragma unroll
    for (int md = 0; md < 2; ++md)
#pragma unroll
      for (int nt = 0; nt < 2; ++nt)
#pragma unroll
        for (int g = 0; g < 4; ++g) {
          const f16v& a = acc[2 * md + nt];
          const f4v v = {a[4 * g], a[4 * g + 1], a[4 * g + 2], a[4 * g + 3]};
          *(LAS f4v*)(L + lo + (32 * nt + (lane & 31)) * 272 + (32 * md + 8 * g + 4 * hh) * 4) = v;
        }
    asm volatile("s_waitcnt vmcnt(0)" : "+v"(fin_ga), "+v"(fin_om), "+v"(fin_b2)::"memory");
    for (u32 sp = 0; sp < SPIN_CAP && __any(max(max4(fin_ga), fin_om) == POISON); ++sp) {
      ld16s(fin_ga, go, sw, 0);
      asm volatile("buffer_load_dword %0, %1, %2, 0 offen sc1" : "=v"(fin_om) : "v"(O_OMG + fs * 4), "s"(sw) : "memory");
      asm volatile("s_waitcnt vmcnt(0)" : "+v"(fin_ga), "+v"(fin_om)::"memory");
    }
    STAMP(12);
  }
  WAITL();
  __builtin_amdgcn_s_barrier();
  asm volatile("" ::: "memory");
  {
    const int sl = tid >> 3;
    const u32 ro = L_G + sl * 272 + (tid & 7) * 32;
    f4v s0v = *(LAS f4v*)(L + ro), s1v = *(LAS f4v*)(L + ro + 16);
#pragma unroll
    for (int l = 1; l < 4; ++l) { s0v += *(LAS f4v*)(L + ro + l * 32768); s1v += *(LAS f4v*)(L + ro + l * 32768 + 16); }
    const float b2 = blo(fin_b2), om = __uint_as_float(fin_om);
    v4u o;
#pragma unroll
    for (int i = 0; i < 4; ++i) {
      const float e0 = i < 2 ? s0v[2 * i] : s1v[2 * i - 4], e1 = i < 2 ? s0v[2 * i + 1] : s1v[2 * i - 3];
      const u32 y = pk(e0, e1);
      const u32 yb = pk(blo(y) + b2, bhi(y) + b2);
      const u32 m = pk(om * blo(yb), om * bhi(yb));
      o[i] = pk(blo(fin_ga[i]) + blo(m), bhi(fin_ga[i]) + bhi(m));
    }
    *(v4u*)(p.out + (size_t)(64 * st + sl) * 512 + dt * 64 + (tid & 7) * 8) = o;
  }
}

extern "C" void kernel_launch(void* const* d_in, const int* in_sizes, int n_in, void* d_out, int out_size, void* d_ws, size_t ws_size,
                              hipStream_t stream) {
  Params p;
  p.x = (const u16*)d_in[0]; p.wr = (const u16*)d_in[1]; p.wi = (const u16*)d_in[2];
  p.w1 = (const u16*)d_in[4]; p.b1 = (const u16*)d_in[5]; p.w2 = (const u16*)d_in[6]; p.b2 = (const u16*)d_in[7]; p.gw = (const u16*)d_in[8];
  p.out = (u16*)d_out;
  p.ws = (char*)d_ws + ((ws_size - WS_BYTES) & ~(size_t)0xFFFFF);
  (void)hipFuncSetAttribute((const void*)fused_kernel, hipFuncAttributeMaxDynamicSharedMemorySize, LDS_BYTES);
  fused_kernel<<<256, 512, LDS_BYTES, stream>>>(p);
}
```

```cpp
#define DIAG
#define AMP_ROLE 2
#define AMP_IDX 0
#include <hip/hip_runtime.h>
#include <stdint.h>

typedef unsigned u32;
typedef unsigned long long u64;
typedef unsigned short u16;
typedef u32 v4u __attribute__((ext_vector_type(4)));
typedef u32 v2u __attribute__((ext_vector_type(2)));
typedef int v4i __attribute__((ext_vector_type(4)));
typedef short s4v __attribute__((ext_vector_type(4)));
typedef short s8v __attribute__((ext_vector_type(8)));
typedef float f2v __attribute__((ext_vector_type(2)));
typedef float f4v __attribute__((ext_vector_type(4)));
typedef float f16v __attribute__((ext_vector_type(16)));
typedef __bf16 b2v __attribute__((ext_vector_type(2)));
#define LAS __attribute__((address_space(3)))
#define DI __device__ __forceinline__
typedef __amdgpu_buffer_rsrc_t rsrc_t;

constexpr u32 O_EPOCH = 0, O_F1 = 4096, O_F2 = 32768, O_XN = 65536, O_VN = 73728, O_OMG = 81920, O_DIAG = 131072;
constexpr u32 O_H = 1u << 20, O_X1 = 3u << 20, O_V = 5u << 20, O_GA = 7u << 20, O_U = 9u << 20, WS_BYTES = 11u << 20;
constexpr u32 POISON = 0xFFFFFFFFu;
constexpr int AUX_SC1 = 16;
constexpr u32 SPIN_CAP = 200000;
constexpr u32 L_G = 0;
constexpr u32 L_S = 131072;
constexpr u32 L_X = L_S, L_HR = L_S + 8704, L_T = L_S + 17408;
constexpr u32 L_E = L_S, L_W = L_S + 8704, L_O = L_S + 17408;
constexpr u32 L_U = L_S;
constexpr u32 L_C = L_S + 26624;
constexpr u32 LDS_BYTES = 163840;
enum { C_H1 = 0, C_F1 = 1, C_OK = 2  , C_P2A = 6, C_P2B = 7, C_P3A = 8, C_P3B = 9, C_R1 = 10, C_R2 = 11, C_F2 = 12, C_TAG = 14  , C_G = 16  , C_NY = 24  , C_N = 32 };

struct Params {
  const u16 *x, *wr, *wi, *w1, *b1, *w2, *b2, *gw;
  u16* out;
  char* ws;
};

DI u32 pk(float a, float b) { f2v v = {a, b}; return __builtin_bit_cast(u32, __builtin_convertvector(v, b2v)); }
DI float blo(u32 v) { return __uint_as_float(v << 16); }
DI float bhi(u32 v) { return __uint_as_float(v & 0xFFFF0000u); }
DI float rbf(float a) { return blo(pk(a, a)); }
DI float cosr(int n, float inv) { return __builtin_amdgcn_cosf((float)n * inv); }
DI float sinr(int n, float inv) { return __builtin_amdgcn_sinf((float)n * inv); }
DI u32 san(u32 v) { return v < 0xFFFFFFFEu ? v : 0xFFFFFFFEu; }
DI v4u san4(v4u v) { v4u r = {san(v.x), san(v.y), san(v.z), san(v.w)}; return r; }
DI u32 max4(v4u v) { return max(max(v.x, v.y), max(v.z, v.w)); }
DI f4v mfma16(v4u a, v4u b, f4v c) { return __builtin_amdgcn_mfma_f32_16x16x32_bf16(__builtin_bit_cast(s8v, a), __builtin_bit_cast(s8v, b), c, 0, 0, 0); }
DI f16v mfma32(v4u a, v4u b, f16v c) { return __builtin_amdgcn_mfma_f32_32x32x16_bf16(__builtin_bit_cast(s8v, a), __builtin_bit_cast(s8v, b), c, 0, 0, 0); }
DI v2u trd(LAS char* L, u32 off) { return __builtin_bit_cast(v2u, __builtin_amdgcn_ds_read_tr16_b64_v4i16((LAS s4v*)(L + off))); }
DI v4u trd2(LAS char* L, u32 o0, u32 o1) { v2u a = trd(L, o0), b = trd(L, o1); v4u r = {a.x, a.y, b.x, b.y}; return r; }
template <int C> DI float dppadd(float v) { return v + __int_as_float(__builtin_amdgcn_update_dpp(0, __float_as_int(v), C, 0xF, 0xF, true)); }
DI float wsum(float v) {
  v = dppadd<0xB1>(v); v = dppadd<0x4E>(v); v = dppadd<0x141>(v); v = dppadd<0x140>(v);
  auto a = __builtin_amdgcn_permlane16_swap(__float_as_uint(v), __float_as_uint(v), false, false);
  v = __uint_as_float(a[0]) + __uint_as_float(a[1]);
  auto b = __builtin_amdgcn_permlane32_swap(__float_as_uint(v), __float_as_uint(v), false, false);
  return __uint_as_float(b[0]) + __uint_as_float(b[1]);
}
DI u64 mix64(u64 z) { z += 0x9E3779B97F4A7C15ull; z = (z ^ (z >> 30)) * 0xBF58476D1CE4E5B9ull; z = (z ^ (z >> 27)) * 0x94D049BB133111EBull; return z ^ (z >> 31); }
#define WAITL() asm volatile("s_waitcnt lgkmcnt(0)" ::: "memory")
template <int N> DI void waitv() { asm volatile("s_waitcnt vmcnt(%0)" ::"n"(N) : "memory"); }
#define SCHED() __builtin_amdgcn_sched_barrier(0)
DI u32 larrive(LAS char* L, int c, int lane) {
  WAITL();
  u32 old = 0;
  if (lane == 0) old = __hip_atomic_fetch_add((LAS u32*)(L + L_C) + c, 1u, __ATOMIC_RELAXED, __HIP_MEMORY_SCOPE_WORKGROUP);
  return __builtin_amdgcn_readfirstlane(old);
}
template <int SL = 0> DI void lwait(LAS char* L, int c, u32 n) {
  volatile LAS u32* p = (volatile LAS u32*)(L + L_C) + c;
  _Pragma("unroll 1") for (u32 s = 0; (u32)__builtin_amdgcn_readfirstlane(*p) < n && s < 40u * SPIN_CAP; ++s) { if (SL) __builtin_amdgcn_s_sleep(SL); }
  asm volatile("" ::: "memory");
}
DI void lsync(LAS char* L, int c, int lane) { larrive(L, c, lane); lwait(L, c, 4); }

#define STAMP(i) ts[i] = __builtin_amdgcn_s_memrealtime()

template <int SGN> DI void dft8(const float (&ar)[8], const float (&ai)[8], float (&yr)[8], float (&yi)[8]) {
  const float R = 0.70710678118654752f, s = (float)SGN;
  float br[8], bi[8];
#pragma unroll
  for (int i = 0; i < 4; ++i) { br[i] = ar[i] + ar[i + 4]; bi[i] = ai[i] + ai[i + 4]; }
  br[4] = ar[0] - ar[4]; bi[4] = ai[0] - ai[4];
  { float r = ar[1] - ar[5], m = ai[1] - ai[5]; br[5] = R * (r - s * m); bi[5] = R * (m + s * r); }
  { float r = ar[2] - ar[6], m = ai[2] - ai[6]; br[6] = -s * m; bi[6] = s * r; }
  { float r = ar[3] - ar[7], m = ai[3] - ai[7]; br[7] = -R * (r + s * m); bi[7] = R * (s * r - m); }
#pragma unroll
  for (int h = 0; h < 2; ++h) {
    const int o = 4 * h;
    float er = br[o] + br[o + 2], ei = bi[o] + bi[o + 2], fr = br[o] - br[o + 2], fi = bi[o] - bi[o + 2];
    float gr = br[o + 1] + br[o + 3], gi = bi[o + 1] + bi[o + 3], hr = br[o + 1] - br[o + 3], hi = bi[o + 1] - bi[o + 3];
    yr[h] = er + gr; yi[h] = ei + gi; yr[h + 4] = er - gr; yi[h + 4] = ei - gi;
    yr[h + 2] = fr - s * hi; yi[h + 2] = fi + s * hr; yr[h + 6] = fr + s * hi; yi[h + 6] = fi - s * hr;
  }
}

struct P2C { v4u f1r, f1i, fr, fi, gr, gi, g1r, g1i; float tc[4], ts[4], uc[4], us[4]; };
DI v4u p2_column(const P2C& c, v4u in, v4u wv) {
  const f4v z = {0.f, 0.f, 0.f, 0.f};
  f4v r = mfma16(in, c.f1r, z), m = mfma16(in, c.f1i, z);
  v4u t;
#pragma unroll
  for (int i = 0; i < 4; ++i) t[i] = pk(r[i] * c.tc[i] + m[i] * c.ts[i], m[i] * c.tc[i] - r[i] * c.ts[i]);
  r = mfma16(c.fr, t, z); m = mfma16(c.fi, t, z);
#pragma unroll
  for (int i = 0; i < 4; ++i) { const float a = blo(wv[i]), b = bhi(wv[i]); t[i] = pk(r[i] * a - m[i] * b, r[i] * b + m[i] * a); }
  r = mfma16(t, c.gr, z); m = mfma16(t, c.gi, z);
#pragma unroll
  for (int i = 0; i < 4; ++i) t[i] = pk(r[i] * c.uc[i] - m[i] * c.us[i], m[i] * c.uc[i] + r[i] * c.us[i]);
  r = mfma16(c.g1r, t, z); m = mfma16(c.g1i, t, z);
#pragma unroll
  for (int i = 0; i < 4; ++i) t[i] = san(pk(r[i], m[i]));
  return t;
}

DI void dma16(u32 ldsaddr, u32 voff, v4i srd, u32 soff) {
  asm volatile("s_mov_b32 m0, %0\n\ts_nop 0\n\tbuffer_load_dwordx4 %1, %2, %3 offen lds" ::"s"(ldsaddr), "v"(voff), "s"(srd), "s"(soff) : "memory");
}
DI void ld16s(v4u& d, u32 voff, v4i srd, u32 soff) { asm volatile("buffer_load_dwordx4 %0, %1, %2, %3 offen sc1" : "=v"(d) : "v"(voff), "s"(srd), "s"(soff) : "memory"); }
DI void ld8s(v2u& d, u32 voff, v4i srd, u32 soff) { asm volatile("buffer_load_dwordx2 %0, %1, %2, %3 offen sc1" : "=v"(d) : "v"(voff), "s"(srd), "s"(soff) : "memory"); }
DI void ld8(v2u& d, u32 voff, v4i srd, u32 soff) { asm volatile("buffer_load_dwordx2 %0, %1, %2, %3 offen" : "=v"(d) : "v"(voff), "s"(srd), "s"(soff) : "memory"); }
DI void st16s(v4u d, u32 voff, v4i srd, u32 soff) { asm volatile("buffer_store_dwordx4 %0, %1, %2, %3 offen sc1\n\ts_nop 1" ::"v"(d), "v"(voff), "s"(srd), "s"(soff) : "memory"); }
DI void st8s(v2u d, u32 voff, v4i srd, u32 soff) { asm volatile("buffer_store_dwordx2 %0, %1, %2, %3 offen sc1\n\ts_nop 1" ::"v"(d), "v"(voff), "s"(srd), "s"(soff) : "memory"); }
template <int N> DI void waitv2(v4u& a, v4u& b) { asm volatile("s_waitcnt vmcnt(%2)" : "+v"(a), "+v"(b) : "n"(N) : "memory"); }
DI v4i mksrd(const void* p, u32 bytes) { const u64 a = (u64)p; v4i r = {(int)(u32)a, (int)((u32)(a >> 32) & 0xFFFFu), (int)bytes, 0x00020000}; return r; }

constexpr int RING = 6;
constexpr int g1_extra(int k) { return (k == 3 ? 8 : 0) + (k == 25 ? 4 : 0) + (k >= 26 && k <= 29 ? 4 : 0) + (k == 30 ? 4 : 0); }
constexpr int g1_after(int k) {
  int n = 0;
  if (k < RING) n += 2 * (RING - 1 - k);
  else n += g1_extra(k - RING);
  for (int j = (k < RING ? 0 : k - RING + 1); j < k; ++j) n += (j + RING < 32 ? 2 : 0) + g1_extra(j);
  return n;
}
constexpr int g2_extra(int k) { return k == 22 ? 3 : 0; }
constexpr int g2_after(int k) {
  int n = 0;
  if (k < RING) n += 2 * (RING - 1 - k);
  else n += g2_extra(k - RING);
  for (int j = (k < RING ? 0 : k - RING + 1); j < k; ++j) n += (j + RING < 28 ? 2 : 0) + g2_extra(j);
  return n;
}

template <int K> struct IC { static constexpr int v = K; };
template <int B, int E, class Fn> DI void sfor(Fn&& f) { if constexpr (B < E) { f(IC<B>{}); sfor<B + 1, E>(f); } }

__global__ void __launch_bounds__(512, 2) fused_kernel(Params p) {
  extern __shared__ __attribute__((aligned(16))) char smem[];
  LAS char* L = (LAS char*)smem;
  const int tid = threadIdx.x, lane = tid & 63, wv = __builtin_amdgcn_readfirstlane(tid >> 6), w = wv & 3;
  const int b = blockIdx.x, xl = b & 7, idx = b >> 3;
  const int dtl = idx & 3, dt = 4 * (xl >> 2) + dtl, grp = xl & 3;
  const int st = 8 * grp + (idx >> 2);
  u64 ts[16];
#pragma unroll
  for (int i = 0; i < 16; ++i) ts[i] = 0;
  STAMP(0);
  const u64 clk0 = __builtin_amdgcn_s_memtime();
  const rsrc_t rw = __builtin_amdgcn_make_buffer_rsrc((void*)p.ws, 0, (int)WS_BYTES, 0x00020000);
  asm volatile("" ::"s"(p.x), "s"(p.wr), "s"(p.wi), "s"(p.w1), "s"(p.b1), "s"(p.w2), "s"(p.b2), "s"(p.gw), "s"(p.out), "s"(p.ws));
  v4u fin_ga; u32 fin_om, fin_b2;

  if (wv < 4) {
    __builtin_amdgcn_s_setprio(3);
    const int s0 = b, q = lane >> 4, c = lane & 15;
    const rsrc_t rx = __builtin_amdgcn_make_buffer_rsrc((void*)p.x, 0, 2048 * 512 * 2, 0x00020000);
    const v4u x0 = __builtin_amdgcn_raw_buffer_load_b128(rx, lane * 16, (s0 + 512 * w) * 1024, 0);
    const v4u x1 = __builtin_amdgcn_raw_buffer_load_b128(rx, lane * 16, (s0 + 512 * w + 256) * 1024, 0);
    const v4u gwv = *(const v4u*)(p.gw + lane * 8);
    const v2u epv = __builtin_amdgcn_raw_buffer_load_b64(rw, O_EPOCH, 0, 0);
    {
      const v4u P = {POISON, POISON, POISON, POISON};
      __builtin_amdgcn_raw_buffer_store_b128(P, rw, lane * 16, O_X1 + (2 * w) * 262144 + s0 * 1024, AUX_SC1);
      __builtin_amdgcn_raw_buffer_store_b128(P, rw, lane * 16, O_X1 + (2 * w + 1) * 262144 + s0 * 1024, AUX_SC1);
      __builtin_amdgcn_raw_buffer_store_b128(P, rw, lane * 16, O_V + b * 8192 + w * 2048, AUX_SC1);
      __builtin_amdgcn_raw_buffer_store_b128(P, rw, lane * 16, O_V + b * 8192 + w * 2048 + 1024, AUX_SC1);
      __builtin_amdgcn_raw_buffer_store_b128(P, rw, lane * 16, O_GA + b * 8192 + w * 2048, AUX_SC1);
      __builtin_amdgcn_raw_buffer_store_b128(P, rw, lane * 16, O_GA + b * 8192 + w * 2048 + 1024, AUX_SC1);
      if (w == 0 && lane < 24)
        __builtin_amdgcn_raw_buffer_store_b32(POISON, rw, lane < 8 ? O_XN + lane * 1024 + s0 * 4 : (lane < 16 ? O_VN : O_OMG) + b * 32 + (lane & 7) * 4, 0, AUX_SC1);
    }
    if (tid < C_N) ((LAS u32*)(L + L_C))[tid] = 0;
    WAITL();
    __builtin_amdgcn_s_barrier();
    float xa[8], xb[8];
#pragma unroll
    for (int i = 0; i < 4; ++i) { xa[2 * i] = blo(x0[i]); xa[2 * i + 1] = bhi(x0[i]); xb[2 * i] = blo(x1[i]); xb[2 * i + 1] = bhi(x1[i]); }
    STAMP(1);
    *(LAS v4u*)(L + L_X + (2 * w) * 1088 + lane * 16) = x0;
    *(LAS v4u*)(L + L_X + (2 * w + 1) * 1088 + lane * 16) = x1;
    {
      float sa = ((xa[0] + xa[1]) + (xa[2] + xa[3])) + ((xa[4] + xa[5]) + (xa[6] + xa[7]));
      float sb = ((xb[0] + xb[1]) + (xb[2] + xb[3])) + ((xb[4] + xb[5]) + (xb[6] + xb[7]));
      const float ma = wsum(sa) * (1.f / 512.f), mb = wsum(sb) * (1.f / 512.f);
      float va = 0.f, vb = 0.f;
#pragma unroll
      for (int i = 0; i < 8; ++i) { const float da = xa[i] - ma, db = xb[i] - mb; va += da * da; vb += db * db; }
      va = wsum(va) * (1.f / 512.f); vb = wsum(vb) * (1.f / 512.f);
      const float ra = __builtin_amdgcn_rcpf(rbf(__builtin_amdgcn_sqrtf(rbf(rbf(va) + 1e-5f))));
      const float rb = __builtin_amdgcn_rcpf(rbf(__builtin_amdgcn_sqrtf(rbf(rbf(vb) + 1e-5f))));
      const float na = -rbf(ma) * ra, nb = -rbf(mb) * rb;
      v4u ha, hb;
#pragma unroll
      for (int i = 0; i < 4; ++i) {
        ha[i] = pk(xa[2 * i] * ra + na, xa[2 * i + 1] * ra + na);
        hb[i] = pk(xb[2 * i] * rb + nb, xb[2 * i + 1] * rb + nb);
      }
      *(LAS v4u*)(L + L_HR + (2 * w) * 1088 + lane * 16) = ha;
      *(LAS v4u*)(L + L_HR + (2 * w + 1) * 1088 + lane * 16) = hb;
    }
    lsync(L, C_H1, lane);
    STAMP(11);
    {
      const u32 ta = L_HR + (c >> 2) * 1088 + (128 * w + 16 * q + 4 * (c & 3)) * 2;
      const v4u h0 = trd2(L, ta, ta + 4 * 1088), h1 = trd2(L, ta + 128, ta + 128 + 4 * 1088);
      const u32 vo = (lane >> 5) * 131072 + (lane & 31) * 16;
      __builtin_amdgcn_raw_buffer_store_b128(h0, rw, vo, O_H + (4 * w) * 131072 + s0 * 512, AUX_SC1);
      __builtin_amdgcn_raw_buffer_store_b128(h1, rw, vo, O_H + (4 * w + 2) * 131072 + s0 * 512, AUX_SC1);
    }
    STAMP(2);
    float om_keep;
    {
      float ga = 0.f, gb = 0.f;
#pragma unroll
      for (int i = 0; i < 4; ++i) {
        const float g0 = blo(gwv[i]), g1 = bhi(gwv[i]);
        const u32 pa = pk(xa[2 * i] * g0, xa[2 * i + 1] * g1), pb = pk(xb[2 * i] * g0, xb[2 * i + 1] * g1);
        ga += blo(pa) + bhi(pa); gb += blo(pb) + bhi(pb);
      }
      ga = rbf(wsum(ga)); gb = rbf(wsum(gb));
      const float gs = (lane & 1) ? gb : ga;
      const float g = rbf(__builtin_amdgcn_rcpf(1.f + __builtin_amdgcn_exp2f(-1.44269504088896f * gs)));
      om_keep = rbf(1.f - g);
      if (lane < 2) ((LAS float*)(L + L_C))[C_G + 2 * w + lane] = g;
    }
    v4u f64r[2], f64i[2], a8;
    float twc[4], tws[4];
    {
      const int k1 = 16 * w + c;
#pragma unroll
      for (int ks = 0; ks < 2; ++ks)
#pragma unroll
        for (int j = 0; j < 4; ++j) {
          const int a = 32 * ks + 8 * q + 2 * j, n0 = (a * k1) & 63, n1 = ((a + 1) * k1) & 63;
          f64r[ks][j] = pk(cosr(n0, 1.f / 64), cosr(n1, 1.f / 64));
          f64i[ks][j] = pk(-sinr(n0, 1.f / 64), -sinr(n1, 1.f / 64));
        }
#pragma unroll
      for (int r = 0; r < 4; ++r) {
        const int bb = 4 * (q & 1) + r, n = (bb * (c >> 2)) & 7;
        const float cs = cosr(n, 0.125f), sn = sinr(n, 0.125f);
        const u32 e = (c & 1) ? pk(-sn, cs) : pk(cs, sn);
        a8[r] = (((c >> 1) & 1) == (q >> 1)) ? e : 0u;
        twc[r] = cosr(bb * k1, 1.f / 512); tws[r] = sinr(bb * k1, 1.f / 512);
      }
    }
    const u64 tag = mix64(((u64)(u32)__builtin_amdgcn_readfirstlane(epv.y) << 32) | (u32)__builtin_amdgcn_readfirstlane(epv.x));
    if (lane == 0) { const v2u tg = {(u32)tag, (u32)(tag >> 32)}; *(LAS v2u*)(L + L_C + C_TAG * 4) = tg; }
    STAMP(12);
    waitv<0>();
    STAMP(3);
    if (larrive(L, C_F1, lane) == 3 && lane < 8) {
      const v2u tg = {(u32)tag, (u32)(tag >> 32)};
      __builtin_amdgcn_raw_buffer_store_b64(tg, rw, O_F1 + lane * 2048 + b * 8, 0, AUX_SC1);
    }
    const int j1 = xl, kb = idx;
    v4u wq0, wq1;
    {
      const rsrc_t rr = __builtin_amdgcn_make_buffer_rsrc((void*)p.wr, 0, 2048 * 257 * 2, 0x00020000);
      const rsrc_t ri = __builtin_amdgcn_make_buffer_rsrc((void*)p.wi, 0, 2048 * 257 * 2, 0x00020000);
      const u32 o = (u32)(j1 + 8 * (64 * w + lane)) * 514u + kb * 16;
      wq0 = __builtin_amdgcn_raw_buffer_load_b128(rr, o, 0, 0);
      wq1 = __builtin_amdgcn_raw_buffer_load_b128(ri, o, 0, 0);
    }
    {
      float yr[8], yi[8], nr[8], ni[8];
      const f4v z = {0.f, 0.f, 0.f, 0.f};
      const u32 xo = L_X + ((c >> 1) & 1) * 1088 + (8 * q + (c >> 2)) * 16 + 8 * (c & 1);
      v4u a8n = a8; a8n[1] ^= 0x80008000u; a8n[3] ^= 0x80008000u;
#pragma unroll
      for (int pi = 0; pi < 4; ++pi) {
        const v4u A0 = trd2(L, xo + pi * 2176, xo + pi * 2176 + 64), A1 = trd2(L, xo + pi * 2176 + 512, xo + pi * 2176 + 576);
        f4v r = mfma16(A0, f64r[0], z), m = mfma16(A0, f64i[0], z);
        r = mfma16(A1, f64r[1], r); m = mfma16(A1, f64i[1], m);
        v4u t;
#pragma unroll
        for (int i = 0; i < 4; ++i) t[i] = pk(r[i] * twc[i] + m[i] * tws[i], m[i] * twc[i] - r[i] * tws[i]);
        const f4v d = mfma16(a8, t, z);
        yr[2 * pi] = d[0]; yi[2 * pi] = d[1]; yr[2 * pi + 1] = d[2]; yi[2 * pi + 1] = d[3];
        if (w == 0) { const f4v e = mfma16(a8n, t, z); nr[2 * pi] = e[0]; ni[2 * pi] = e[1]; nr[2 * pi + 1] = e[2]; ni[2 * pi + 1] = e[3]; }
      }
      float zr[8], zi[8];
      dft8<-1>(yr, yi, zr, zi);
#pragma unroll
      for (int j = 0; j < 8; ++j) *(LAS u32*)(L + L_T + w * 2048 + j * 256 + lane * 4) = san(pk(zr[j], zi[j]));
      const v4u o0 = *(LAS v4u*)(L + L_T + w * 2048 + lane * 16), o1 = *(LAS v4u*)(L + L_T + w * 2048 + 1024 + lane * 16);
      STAMP(13);
      lwait(L, C_F1, 4);
      const u32 vo = q * 262144 + (((lane >> 2) & 3) * 64 + (lane & 3) * 4) * 4;
      __builtin_amdgcn_raw_buffer_store_b128(o0, rw, vo, O_X1 + s0 * 1024 + w * 64, AUX_SC1);
      __builtin_amdgcn_raw_buffer_store_b128(o1, rw, vo, O_X1 + 4 * 262144 + s0 * 1024 + w * 64, AUX_SC1);
      if (w == 0) {
        dft8<-1>(nr, ni, zr, zi);
        if (lane == 0) {
#pragma unroll
          for (int j = 0; j < 8; ++j) __builtin_amdgcn_raw_buffer_store_b32(san(pk(zr[j], zi[j])), rw, O_XN + j * 1024 + s0 * 4, 0, AUX_SC1);
        }
      }
    }
    STAMP(4);
    const u32 epos = ((w * 16 + c) * 4 + q) * 4;
    P2C pc;
    {
#pragma unroll
      for (int i = 0; i < 4; ++i) {
        const int e = 4 * q + i;
        { const int n = (e * (8 * c + j1)) & 127; const float cs = cosr(n, 1.f / 128), sn = sinr(n, 1.f / 128); pc.f1r[i] = pk(cs, sn); pc.f1i[i] = pk(-sn, cs); }
        { const int n = (e * c) & 15; const float cs = cosr(n, 1.f / 16), sn = sinr(n, 1.f / 16); pc.fr[i] = pk(cs, sn); pc.fi[i] = pk(-sn, cs); pc.gr[i] = pk(cs, -sn); pc.gi[i] = pk(sn, cs); }
        { const int n = (c * (8 * e + j1)) & 127; const float cs = cosr(n, 1.f / 128), sn = sinr(n, 1.f / 128); pc.g1r[i] = pk(cs, -sn); pc.g1i[i] = pk(sn, cs); }
        pc.tc[i] = cosr(e * (8 * c + j1), 1.f / 2048); pc.ts[i] = sinr(e * (8 * c + j1), 1.f / 2048);
        pc.uc[i] = cosr(c * (8 * e + j1), 1.f / 2048); pc.us[i] = sinr(c * (8 * e + j1), 1.f / 2048);
      }
#pragma unroll
      for (int i = 0; i < 4; ++i) {
        u32 lo = __builtin_amdgcn_perm(wq1[i], wq0[i], 0x05040100u), hi = __builtin_amdgcn_perm(wq1[i], wq0[i], 0x07060302u);
        if (i == 0 && kb == 0) lo = pk(0.5f * blo(lo), 0.5f * bhi(lo));
        *(LAS u32*)(L + L_W + (2 * i) * 1024 + epos) = lo;
        *(LAS u32*)(L + L_W + (2 * i + 1) * 1024 + epos) = hi;
      }
    }
    {
      const u32 fo = O_F1 + xl * 2048 + (64 * w + lane) * 8;
      _Pragma("unroll 1") for (u32 s = 0; s < SPIN_CAP; ++s) {
        asm volatile("" ::: "memory");
        const v2u f = __builtin_amdgcn_raw_buffer_load_b64(rw, fo, 0, AUX_SC1);
        if (__all(f.x == (u32)tag && f.y == (u32)(tag >> 32))) break;
        __builtin_amdgcn_s_sleep(2);
      }
      asm volatile("" ::: "memory");
      if (lane == 0) { ((volatile LAS u32*)(L + L_C))[C_OK + w] = 1u; }
      STAMP(5);
      lwait(L, C_OK + 0, 1); lwait(L, C_OK + 1, 1); lwait(L, C_OK + 2, 1); lwait(L, C_OK + 3, 1);
    }
    if (b == 0 && tid == 0) *(u64*)(p.ws + O_EPOCH) = tag;
    if (lane < 2) __builtin_amdgcn_raw_buffer_store_b32(__float_as_uint(om_keep), rw, O_OMG + (s0 + 256 * (2 * w + lane)) * 4, 0, AUX_SC1);
    {
      const u32 xo = O_X1 + j1 * 262144 + (64 * w + lane) * 1024 + kb * 32;
      v4u e0 = __builtin_amdgcn_raw_buffer_load_b128(rw, xo, 0, AUX_SC1), e1 = __builtin_amdgcn_raw_buffer_load_b128(rw, xo + 16, 0, AUX_SC1);
      _Pragma("unroll 1") for (u32 s = 0; s < SPIN_CAP; ++s) {
        asm volatile("" ::: "memory");
        const bool bad = max(max4(e0), max4(e1)) == POISON;
        if (!__any(bad)) break;
        __builtin_amdgcn_s_sleep(1);
        if (bad) { e0 = __builtin_amdgcn_raw_buffer_load_b128(rw, xo, 0, AUX_SC1); e1 = __builtin_amdgcn_raw_buffer_load_b128(rw, xo + 16, 0, AUX_SC1); }
      }
      STAMP(6);
#pragma unroll
      for (int i = 0; i < 4; ++i) { *(LAS u32*)(L + L_E + i * 1024 + epos) = e0[i]; *(LAS u32*)(L + L_E + (4 + i) * 1024 + epos) = e1[i]; }
      lsync(L, C_P2A, lane);
      const v4u i0 = *(LAS v4u*)(L + L_E + (2 * w) * 1024 + lane * 16), i1 = *(LAS v4u*)(L + L_E + (2 * w + 1) * 1024 + lane * 16);
      const v4u w0 = *(LAS v4u*)(L + L_W + (2 * w) * 1024 + lane * 16), w1 = *(LAS v4u*)(L + L_W + (2 * w + 1) * 1024 + lane * 16);
      *(LAS v4u*)(L + L_O + (2 * w) * 1024 + lane * 16) = p2_column(pc, i0, w0);
      *(LAS v4u*)(L + L_O + (2 * w + 1) * 1024 + lane * 16) = p2_column(pc, i1, w1);
      lsync(L, C_P2B, lane);
      v4u o0, o1;
#pragma unroll
      for (int i = 0; i < 4; ++i) { o0[i] = *(LAS u32*)(L + L_O + i * 1024 + epos); o1[i] = *(LAS u32*)(L + L_O + (4 + i) * 1024 + epos); }
      STAMP(14);
      const u32 vo = O_V + (64 * w + lane) * 8192 + j1 * 1024 + kb * 32;
      __builtin_amdgcn_raw_buffer_store_b128(o0, rw, vo, 0, AUX_SC1);
      __builtin_amdgcn_raw_buffer_store_b128(o1, rw, vo + 16, 0, AUX_SC1);
      if (kb == 31 && w == 0) {
        v4u in, wn;
#pragma unroll
        for (int i = 0; i < 4; ++i) {
          const u32 e = 64 * q + 16 * i + c, o = (j1 + 8 * e) * 257 + 256;
          wn[i] = pk(0.5f * blo(p.wr[o]), 0.5f * blo(p.wi[o]));
          in[i] = POISON;
        }
        _Pragma("unroll 1") for (u32 s = 0; s < SPIN_CAP; ++s) {
        asm volatile("" ::: "memory");
#pragma unroll
          for (int i = 0; i < 4; ++i) in[i] = __builtin_amdgcn_raw_buffer_load_b32(rw, O_XN + j1 * 1024 + (64 * q + 16 * i + c) * 4, 0, AUX_SC1);
          if (!__any(max4(in) == POISON)) break;
        }
        const v4u o = p2_column(pc, in, wn);
#pragma unroll
        for (int i = 0; i < 4; ++i) __builtin_amdgcn_raw_buffer_store_b32(o[i], rw, O_VN + (64 * q + 16 * i + c) * 32 + j1 * 4, 0, AUX_SC1);
      }
    }
    STAMP(7);
    {
      v4u gB[4];
      float pcs[2], psn[2];
      u32 ax;
      {
        const int ap = 16 * w + c;
#pragma unroll
        for (int ks = 0; ks < 4; ++ks)
#pragma unroll
          for (int i = 0; i < 4; ++i) {
            const int n = ((16 * ks + 4 * q + i) * ap) & 63;
            gB[ks][i] = pk(cosr(n, 1.f / 64) * 0x1p-19f, -sinr(n, 1.f / 64) * 0x1p-19f);
          }
        const int n = (q * (c >> 1)) & 7;
        const float cs = cosr(n, 0.125f), sn = sinr(n, 0.125f);
        ax = (c & 1) ? pk(sn, cs) : pk(cs, -sn);
#pragma unroll
        for (int e = 0; e < 2; ++e) { pcs[e] = cosr(ap * (2 * q + e), 1.f / 512); psn[e] = sinr(ap * (2 * q + e), 1.f / 512); }
      }
      const u32 vo = O_V + s0 * 8192 + (16 * w + 64 * q + c) * 4;
      u32 vv[8];
#pragma unroll
      for (int j = 0; j < 8; ++j) vv[j] = __builtin_amdgcn_raw_buffer_load_b32(rw, vo + j * 1024, 0, AUX_SC1);
      _Pragma("unroll 1") for (u32 s = 0; s < SPIN_CAP; ++s) {
        asm volatile("" ::: "memory");
        const bool bad = max(max(max(vv[0], vv[1]), max(vv[2], vv[3])), max(max(vv[4], vv[5]), max(vv[6], vv[7]))) == POISON;
        if (!__any(bad)) break;
        __builtin_amdgcn_s_sleep(1);
        if (bad) {
#pragma unroll
          for (int j = 0; j < 8; ++j) vv[j] = __builtin_amdgcn_raw_buffer_load_b32(rw, vo + j * 1024, 0, AUX_SC1);
        }
      }
      STAMP(8);
      float ar[8], ai[8], yr[8], yi[8];
#pragma unroll
      for (int j = 0; j < 8; ++j) { ar[j] = blo(vv[j]); ai[j] = bhi(vv[j]); }
      dft8<1>(ar, ai, yr, yi);
      v4u yq[2];
#pragma unroll
      for (int i = 0; i < 8; ++i) yq[i >> 2][i & 3] = pk(yr[i], yi[i]);
      const f4v z = {0.f, 0.f, 0.f, 0.f};
#pragma unroll
      for (int i = 0; i < 8; ++i) {
        v4u A = {0u, 0u, 0u, 0u};
        A[i & 3] = ax;
        const f4v d = mfma16(A, yq[i >> 2], z);
        *(LAS u32*)(L + L_U + i * 2176 + (2 * q) * 272 + (16 * w + c) * 4) = pk(d[0] * pcs[0] - d[1] * psn[0], d[1] * pcs[0] + d[0] * psn[0]);
        *(LAS u32*)(L + L_U + i * 2176 + (2 * q + 1) * 272 + (16 * w + c) * 4) = pk(d[2] * pcs[1] - d[3] * psn[1], d[3] * pcs[1] + d[2] * psn[1]);
      }
      if (w == 0) {
        v4u n0, n1;
        _Pragma("unroll 1") for (u32 s = 0; s < SPIN_CAP; ++s) {
        asm volatile("" ::: "memory");
          n0 = __builtin_amdgcn_raw_buffer_load_b128(rw, O_VN + s0 * 32, 0, AUX_SC1);
          n1 = __builtin_amdgcn_raw_buffer_load_b128(rw, O_VN + s0 * 32 + 16, 0, AUX_SC1);
          if (!__any(max(max4(n0), max4(n1)) == POISON)) break;
        }
#pragma unroll
        for (int j = 0; j < 4; ++j) { ar[j] = blo(n0[j]); ai[j] = bhi(n0[j]); ar[j + 4] = blo(n1[j]); ai[j + 4] = bhi(n1[j]); }
        dft8<1>(ar, ai, yr, yi);
        if (lane == 0) {
#pragma unroll
          for (int i = 0; i < 8; ++i) ((LAS float*)(L + L_C))[C_NY + i] = yr[i] * 0x1p-19f;
        }
      }
      lsync(L, C_P3B, lane);
      const u32 uo = L_U + (c >> 3) * 2176 + (c & 7) * 272 + q * 16;
#pragma unroll
      for (int pi = 0; pi < 4; ++pi) {
        f4v d = z;
#pragma unroll
        for (int ks = 0; ks < 4; ++ks) d = mfma16(*(LAS v4u*)(L + uo + pi * 4352 + ks * 64), gB[ks], d);
        const int i = 2 * pi + (q >> 1);
        const float ny = ((LAS float*)(L + L_C))[C_NY + i], g = ((LAS float*)(L + L_C))[C_G + i];
        const u32 a01 = pk(d[0] + ny, d[1] - ny), a23 = pk(d[2] + ny, d[3] - ny);
        const v2u o = {san(pk(g * blo(a01), g * bhi(a01))), san(pk(g * blo(a23), g * bhi(a23)))};
        __builtin_amdgcn_raw_buffer_store_b64(o, rw, O_GA + (s0 + 256 * i) * 1024 + ((16 * w + c) * 8 + 4 * (q & 1)) * 2, 0, AUX_SC1);
      }
    }
    STAMP(9);
    {
      const int sl = (tid >> 3) & 63, s = 64 * st + sl;
      fin_b2 = p.b2[s];
      const u32 go = O_GA + s * 1024 + dt * 128 + (tid & 7) * 16;
      _Pragma("unroll 1") for (u32 sp = 0; sp < SPIN_CAP; ++sp) {
        asm volatile("" ::: "memory");
        fin_ga = __builtin_amdgcn_raw_buffer_load_b128(rw, go, 0, AUX_SC1);
        fin_om = __builtin_amdgcn_raw_buffer_load_b32(rw, O_OMG + s * 4, 0, AUX_SC1);
        if (!__any(max(max4(fin_ga), fin_om) == POISON)) break;
        __builtin_amdgcn_s_sleep(4);
      }
    }
    STAMP(10);
  } else {
    __builtin_amdgcn_s_barrier();
    const v4i sw = mksrd(p.ws, WS_BYTES), s2 = mksrd(p.w2, 1792u * 4096u);
    u32 lb = (u32)(size_t)smem + L_G + w * 32768, w2b = (u32)(448 * w) * 4096u + st * 128, rot2 = 7 * dtl;
    const u32 lo = L_G + w * 32768;
    const int hh = lane >> 5, blk = (lane >> 4) & 1, tq = (lane & 15) >> 2, tp = lane & 3;
    const u32 v2off = (lane >> 3) * 4096 + (((lane & 7) ^ (((lane >> 4) & 1) << 2)) * 16);
    auto w2dma = [&](int kl, u32 dst) {
      u32 kg = kl + rot2; kg = kg >= 28 ? kg - 28 : kg;
      const u32 so = w2b + kg * 65536u;
      dma16(lb + dst, v2off, s2, so);
      dma16(lb + dst + 1024, v2off, s2, so + 8 * 4096);
    };
    const u32 t2 = lo + (4 * hh + tq) * 128 + (((2 * blk + (tp >> 1)) ^ (((tq >> 1) & 1) << 2)) * 16) + 8 * (tp & 1);
    v4u wr_[24];
    f16v acc[4];
    v4u ring[2 * RING];
    if (idx < 28) {
      const int ft = 7 * grp + (idx >> 2);
      const v4i s1 = mksrd(p.w1, 2048u * 3584u);
      u32 v1off[4];
#pragma unroll
      for (int j = 0; j < 4; ++j) v1off[j] = ((lane >> 3) ^ (j >> 1)) * 3584 + ((lane & 7) ^ ((j & 1) << 2)) * 16;
      auto w1dma = [&](int bl, int slot) {
        const u32 so = (u32)(64 * w + 8 * ((bl + 2 * dtl) & 7)) * 3584u + ft * 128;
#pragma unroll
        for (int j = 0; j < 8; ++j) dma16(lb + slot * 8192 + j * 1024, v1off[j & 3], s1, so + j * (256u * 3584u));
      };
      const u32 t1 = lo + tq * 1024 + ((hh ^ (tq >> 1)) * 128) + ((((w >> 1) * 4 + 2 * blk + (tp >> 1)) ^ ((tq & 1) << 2)) * 16) + 8 * (tp & 1);
      const u32 t1x = t1 ^ 64u;
      w1dma(0, 0); w1dma(1, 1); STAMP(13); w1dma(2, 2); w1dma(3, 3);
      STAMP(1);
      sfor<0, 3>([&](auto B) {
        constexpr int bl = decltype(B)::v;
        waitv<24>();
#pragma unroll
        for (int k = 0; k < 4; ++k) {
          wr_[8 * bl + 2 * k] = trd2(L, t1 + bl * 8192 + k * 256, t1 + bl * 8192 + k * 256 + 4096);
          wr_[8 * bl + 2 * k + 1] = trd2(L, t1x + bl * 8192 + k * 256, t1x + bl * 8192 + k * 256 + 4096);
        }
        WAITL();
        w1dma(4 + bl, bl);
      });
      STAMP(2);
      lwait<1>(L, C_OK + w, 1);
      STAMP(3);
      auto hload = [&](int k) {
        const int pos = k >> 2, bl = pos < 4 ? pos + 3 : (pos < 7 ? pos - 4 : 7);
        const u32 so = O_H + (u32)(64 * w + 8 * ((bl + 2 * dtl) & 7) + 2 * (k & 3)) * 512u;
        ld16s(ring[2 * (k % RING)], lane * 16, sw, so + (2 * dt + (w & 1)) * 131072);
        ld16s(ring[2 * (k % RING) + 1], lane * 16, sw, so + (2 * dt + ((w & 1) ^ 1)) * 131072);
      };
#pragma unroll
      for (int k = 0; k < RING; ++k) hload(k);
#pragma unroll
      for (int i = 0; i < 4; ++i)
#pragma unroll
        for (int j = 0; j < 16; ++j) acc[i][j] = 0.f;
      v2u bias[4];
      v4u wf[2][2];
      sfor<0, 32>([&](auto K) {
        constexpr int k = decltype(K)::v, pos = k >> 2;
        waitv2<g1_after(k)>(ring[2 * (k % RING)], ring[2 * (k % RING) + 1]);
        if constexpr (k == 0) {
          STAMP(4);
          wf[0][0] = trd2(L, t1 + 3 * 8192, t1 + 3 * 8192 + 4096); wf[0][1] = trd2(L, t1x + 3 * 8192, t1x + 3 * 8192 + 4096);
        }
        constexpr int kn = k + 1, pn = kn >> 2;
        if constexpr (kn < 32 && (pn < 4 || pn == 7)) {
          constexpr u32 o = (pn == 0 || pn == 7 ? 3 : pn - 1) * 8192 + (kn & 3) * 256;
          wf[kn & 1][0] = trd2(L, t1 + o, t1 + o + 4096); wf[kn & 1][1] = trd2(L, t1x + o, t1x + o + 4096);
        }
        const v4u a0 = (pos >= 4 && pos < 7) ? wr_[2 * ((k - 16) & 15) % 24] : wf[k & 1][0];
        const v4u a1 = (pos >= 4 && pos < 7) ? wr_[(2 * ((k - 16) & 15) + 1) % 24] : wf[k & 1][1];
        acc[0] = mfma32(a0, ring[2 * (k % RING)], acc[0]);
        acc[1] = mfma32(a0, ring[2 * (k % RING) + 1], acc[1]);
        acc[2] = mfma32(a1, ring[2 * (k % RING)], acc[2]);
        acc[3] = mfma32(a1, ring[2 * (k % RING) + 1], acc[3]);
        SCHED();
        if constexpr (k + RING < 32) hload(k + RING);
        if constexpr (k == 15) { STAMP(14); }
        if constexpr (k == 3) { WAITL(); w1dma(7, 3); }
        if constexpr (k == 25) {
          const v4i sb = mksrd(p.b1, 1792 * 2);
#pragma unroll
          for (int g = 0; g < 4; ++g) ld8(bias[g], (8 * g + 4 * hh) * 2, sb, (64 * ft + 32 * (w >> 1)) * 2);
        }
        if constexpr (k >= 26 && k <= 30) { w2dma(2 * (k - 26), 2 * (k - 26) * 2048); w2dma(2 * (k - 26) + 1, (2 * (k - 26) + 1) * 2048); }
        SCHED();
      });
      STAMP(5);
#pragma unroll
      for (int l = 1; l < 4; ++l)
#pragma unroll
        for (int g = 0; g < 4; ++g) {
          const f4v v = {acc[l][4 * g], acc[l][4 * g + 1], acc[l][4 * g + 2], acc[l][4 * g + 3]};
          *(LAS f4v*)(L + lo + 20480 + (l - 1) * 4096 + g * 1024 + lane * 16) = v;
        }
      lsync(L, C_R1, lane);
#pragma unroll
      for (int l = 1; l < 4; ++l)
#pragma unroll
        for (int g = 0; g < 4; ++g) {
          const f4v v = *(LAS f4v*)(L + L_G + (w ^ l) * 32768 + 20480 + (l - 1) * 4096 + g * 1024 + lane * 16);
#pragma unroll
          for (int e = 0; e < 4; ++e) acc[0][4 * g + e] += v[e];
        }
      larrive(L, C_R2, lane);
      asm volatile("s_waitcnt vmcnt(20)" : "+v"(bias[0]), "+v"(bias[1]), "+v"(bias[2]), "+v"(bias[3])::"memory");
      v4u uf[2];
#pragma unroll
      for (int g = 0; g < 4; ++g) {
        float o[4];
#pragma unroll
        for (int e = 0; e < 4; ++e) {
          const float v = acc[0][4 * g + e] + ((e & 1) ? bhi(bias[g][e >> 1]) : blo(bias[g][e >> 1]));
          const float t = v * (-2.3022081985f - 0.1029432397f * v * v);
          o[e] = v * __builtin_amdgcn_rcpf(1.f + __builtin_amdgcn_exp2f(t));
        }
        uf[g >> 1][2 * (g & 1)] = pk(o[0], o[1]); uf[g >> 1][2 * (g & 1) + 1] = pk(o[2], o[3]);
      }
      const u32 uo = O_U + (u32)((2 * dt + (w & 1)) * 112 + 4 * ft + 2 * (w >> 1)) * 1024u;
      st16s(uf[0], lane * 16, sw, uo);
      st16s(uf[1], lane * 16, sw, uo + 1024);
      STAMP(6);
      waitv<2>();
#pragma unroll
      for (int k = 0; k < 10; ++k) { wr_[2 * k] = trd2(L, t2 + k * 2048, t2 + k * 2048 + 1024); wr_[2 * k + 1] = trd2(L, (t2 ^ 64u) + k * 2048, (t2 ^ 64u) + k * 2048 + 1024); }
      WAITL();
#pragma unroll
      for (int k = 16; k < 20; ++k) w2dma(k, (k - 16) * 2048);
      waitv<8>();
      if (larrive(L, C_F2, lane) == 3 && lane == 0) st8s(*(LAS v2u*)(L + L_C + C_TAG * 4), 0, sw, O_F2 + (dt * 28 + ft) * 128);
      STAMP(7);
      lwait(L, C_R2, 4);
#pragma unroll
      for (int k = 10; k < 16; ++k) w2dma(k, k * 2048);
#pragma unroll
      for (int k = 20; k < 26; ++k) w2dma(k, (k - 16) * 2048);
      waitv<20>();
    } else {
#pragma unroll
      for (int k = 0; k < 16; ++k) w2dma(k, k * 2048);
      waitv<12>();
#pragma unroll
      for (int k = 0; k < 10; ++k) { wr_[2 * k] = trd2(L, t2 + k * 2048, t2 + k * 2048 + 1024); wr_[2 * k + 1] = trd2(L, (t2 ^ 64u) + k * 2048, (t2 ^ 64u) + k * 2048 + 1024); }
      WAITL();
#pragma unroll
      for (int k = 16; k < 26; ++k) w2dma(k, (k - 16) * 2048);
      waitv<20>();
    }
    asm volatile("" : "+s"(lb), "+s"(w2b), "+s"(rot2));
#pragma unroll
    for (int k = 10; k < 12; ++k) { wr_[2 * k] = trd2(L, t2 + k * 2048, t2 + k * 2048 + 1024); wr_[2 * k + 1] = trd2(L, (t2 ^ 64u) + k * 2048, (t2 ^ 64u) + k * 2048 + 1024); }
    WAITL();
    w2dma(26, 10 * 2048); w2dma(27, 11 * 2048);
    lwait<1>(L, C_OK + 0, 1); lwait<1>(L, C_OK + 1, 1); lwait<1>(L, C_OK + 2, 1); lwait<1>(L, C_OK + 3, 1);
    STAMP(8);
    {
      const u32 fo = O_F2 + (dt * 28 + 7 * w + (lane < 7 ? lane : 6)) * 128;
      const v2u tg = *(LAS v2u*)(L + L_C + C_TAG * 4);
      _Pragma("unroll 1") for (u32 s = 0; s < SPIN_CAP; ++s) {
        asm volatile("" ::: "memory");
        v2u f;
        ld8s(f, fo, sw, 0);
        asm volatile("s_waitcnt vmcnt(0)" : "+v"(f)::"memory");
        if (__all(f.x == tg.x && f.y == tg.y)) break;
        __builtin_amdgcn_s_sleep(1);
      }
    }
    STAMP(9);
    auto uload = [&](int k) {
      u32 kg = k + rot2; kg = kg >= 28 ? kg - 28 : kg;
      const u32 so = O_U + (u32)(2 * dt * 112 + 28 * w + kg) * 1024u;
      ld16s(ring[2 * (k % RING)], lane * 16, sw, so);
      ld16s(ring[2 * (k % RING) + 1], lane * 16, sw, so + 112 * 1024);
    };
#pragma unroll
    for (int k = 0; k < RING; ++k) uload(k);
#pragma unroll
    for (int i = 0; i < 4; ++i)
#pragma unroll
      for (int j = 0; j < 16; ++j) acc[i][j] = 0.f;
    const int fs = 64 * st + ((tid >> 3) & 63);
    const u32 go = O_GA + fs * 1024 + dt * 128 + (tid & 7) * 16;
    {
      v4u wf[2][2];
      sfor<0, 28>([&](auto K) {
        constexpr int k = decltype(K)::v;
        waitv2<g2_after(k)>(ring[2 * (k % RING)], ring[2 * (k % RING) + 1]);
        if constexpr (k == 0) { STAMP(10); }
        constexpr int kn = k + 1;
        if constexpr (kn >= 12 && kn < 28) {
          constexpr u32 o = (kn < 16 ? kn : kn - 16) * 2048;
          wf[kn & 1][0] = trd2(L, t2 + o, t2 + o + 1024); wf[kn & 1][1] = trd2(L, (t2 ^ 64u) + o, (t2 ^ 64u) + o + 1024);
        }
        const v4u b0 = k < 12 ? wr_[(2 * k) % 24] : wf[k & 1][0];
        const v4u b1 = k < 12 ? wr_[(2 * k + 1) % 24] : wf[k & 1][1];
        acc[0] = mfma32(ring[2 * (k % RING)], b0, acc[0]);
        acc[1] = mfma32(ring[2 * (k % RING)], b1, acc[1]);
        acc[2] = mfma32(ring[2 * (k % RING) + 1], b0, acc[2]);
        acc[3] = mfma32(ring[2 * (k % RING) + 1], b1, acc[3]);
        SCHED();
        if constexpr (k + RING < 28) uload(k + RING);
        if constexpr (k == 22) {
          ld16s(fin_ga, go, sw, 0);
          asm volatile("buffer_load_dword %0, %1, %2, 0 offen sc1" : "=v"(fin_om) : "v"(O_OMG + fs * 4), "s"(sw) : "memory");
          const v4i sb = mksrd(p.b2, 2048 * 2);
          asm volatile("buffer_load_ushort %0, %1, %2, 0 offen" : "=v"(fin_b2) : "v"(fs * 2), "s"(sb) : "memory");
        }
        SCHED();
      });
    }
    STAMP(11);
#pragma unroll
    for (int md = 0; md < 2; ++md)
#pragma unroll
      for (int nt = 0; nt < 2; ++nt)
#pragma unroll
        for (int g = 0; g < 4; ++g) {
          const f16v& a = acc[2 * md + nt];
          const f4v v = {a[4 * g], a[4 * g + 1], a[4 * g + 2], a[4 * g + 3]};
          *(LAS f4v*)(L + lo + (32 * nt + (lane & 31)) * 272 + (32 * md + 8 * g + 4 * hh) * 4) = v;
        }
    asm volatile("s_waitcnt vmcnt(0)" : "+v"(fin_ga), "+v"(fin_om), "+v"(fin_b2)::"memory");
    _Pragma("unroll 1") for (u32 sp = 0; sp < SPIN_CAP && __any(max(max4(fin_ga), fin_om) == POISON); ++sp) {
      ld16s(fin_ga, go, sw, 0);
      asm volatile("buffer_load_dword %0, %1, %2, 0 offen sc1" : "=v"(fin_om) : "v"(O_OMG + fs * 4), "s"(sw) : "memory");
      asm volatile("s_waitcnt vmcnt(0)" : "+v"(fin_ga), "+v"(fin_om)::"memory");
    }
    STAMP(12);
  }
  WAITL();
  __builtin_amdgcn_s_barrier();
  asm volatile("" ::: "memory");
  {
    const int sl = tid >> 3;
    const u32 ro = L_G + sl * 272 + (tid & 7) * 32;
    f4v s0v = *(LAS f4v*)(L + ro), s1v = *(LAS f4v*)(L + ro + 16);
#pragma unroll
    for (int l = 1; l < 4; ++l) { s0v += *(LAS f4v*)(L + ro + l * 32768); s1v += *(LAS f4v*)(L + ro + l * 32768 + 16); }
    const float b2 = blo(fin_b2), om = __uint_as_float(fin_om);
    v4u o;
#pragma unroll
    for (int i = 0; i < 4; ++i) {
      const float e0 = i < 2 ? s0v[2 * i] : s1v[2 * i - 4], e1 = i < 2 ? s0v[2 * i + 1] : s1v[2 * i - 3];
      const u32 y = pk(e0, e1);
      const u32 yb = pk(blo(y) + b2, bhi(y) + b2);
      const u32 m = pk(om * blo(yb), om * bhi(yb));
      o[i] = pk(blo(fin_ga[i]) + blo(m), bhi(fin_ga[i]) + bhi(m));
    }
    *(v4u*)(p.out + (size_t)(64 * st + sl) * 512 + dt * 64 + (tid & 7) * 8) = o;
  }
  STAMP(15);
  const u64 clk1 = __builtin_amdgcn_s_memtime();
  if (tid == 0) ((u64*)(p.ws + O_DIAG))[256 * 128 + b] = clk1 - clk0;
  if (lane == 0) {
    u64* d = (u64*)(p.ws + O_DIAG) + (b * 8 + wv) * 16;
#pragma unroll
    for (int i = 0; i < 16; ++i) d[i] = ts[i];
  }
}

__global__ void amp_kernel(const u64* d, u64* sink) {
  __shared__ u64 mn[256], mx[256];
  u64 a = ~0ull, z = 0;
  for (int w = 0; w < 8; ++w) { const u64* e = d + (threadIdx.x * 8 + w) * 16; a = a < e[0] ? a : e[0]; if ((AMP_ROLE == 2 || (w >> 2) == AMP_ROLE) && e[AMP_IDX] > z) z = e[AMP_IDX]; }
  mn[threadIdx.x] = a; mx[threadIdx.x] = z; __syncthreads();
  if (threadIdx.x == 0) { for (int i = 0; i < 256; ++i) { a = a < mn[i] ? a : mn[i]; z = z > mx[i] ? z : mx[i]; }
    u64 dur = (z - a) * 50; if (dur > 500000) dur = 500000; const u64 t0 = __builtin_amdgcn_s_memrealtime(); while (__builtin_amdgcn_s_memrealtime() - t0 < dur) __builtin_amdgcn_s_sleep(8); sink[0] = dur; }
}
extern "C" void kernel_launch(void* const* d_in, const int* in_sizes, int n_in, void* d_out, int out_size, void* d_ws, size_t ws_size,
                              hipStream_t stream) {
  Params p;
  p.x = (const u16*)d_in[0]; p.wr = (const u16*)d_in[1]; p.wi = (const u16*)d_in[2];
  p.w1 = (const u16*)d_in[4]; p.b1 = (const u16*)d_in[5]; p.w2 = (const u16*)d_in[6]; p.b2 = (const u16*)d_in[7]; p.gw = (const u16*)d_in[8];
  p.out = (u16*)d_out;
  p.ws = (char*)d_ws + ((ws_size - WS_BYTES) & ~(size_t)0xFFFFF);
  (void)hipFuncSetAttribute((const void*)fused_kernel, hipFuncAttributeMaxDynamicSharedMemorySize, LDS_BYTES);
  fused_kernel<<<256, 512, LDS_BYTES, stream>>>(p);
  amp_kernel<<<1, 256, 0, stream>>>((const u64*)(p.ws + O_DIAG), (u64*)(p.ws + O_DIAG + 600000));
}
```

```cpp
#include <hip/hip_runtime.h>
#include <stdint.h>

typedef unsigned u32;
typedef unsigned long long u64;
typedef unsigned short u16;
typedef u32 v4u __attribute__((ext_vector_type(4)));
typedef u32 v2u __attribute__((ext_vector_type(2)));
typedef int v4i __attribute__((ext_vector_type(4)));
typedef short s4v __attribute__((ext_vector_type(4)));
typedef short s8v __attribute__((ext_vector_type(8)));
typedef float f2v __attribute__((ext_vector_type(2)));
typedef float f4v __attribute__((ext_vector_type(4)));
typedef float f16v __attribute__((ext_vector_type(16)));
typedef __bf16 b2v __attribute__((ext_vector_type(2)));
#define LAS __attribute__((address_space(3)))
#define DI __device__ __forceinline__
typedef __amdgpu_buffer_rsrc_t rsrc_t;

constexpr u32 O_EPOCH = 0, O_F1 = 4096, O_F2 = 32768, O_XN = 65536, O_VN = 73728, O_OMG = 81920, O_DIAG = 131072;
constexpr u32 O_H = 1u << 20, O_X1 = 3u << 20, O_V = 5u << 20, O_GA = 7u << 20, O_U = 9u << 20, WS_BYTES = 11u << 20;
constexpr u32 POISON = 0xFFFFFFFFu;
constexpr int AUX_SC1 = 16;
constexpr u32 SPIN_CAP = 200000;
constexpr u32 L_G = 0;
constexpr u32 L_S = 131072;
constexpr u32 L_X = L_S, L_HR = L_S + 8704, L_T = L_S + 17408;
constexpr u32 L_E = L_S, L_W = L_S + 8704, L_O = L_S + 17408;
constexpr u32 L_U = L_S;
constexpr u32 L_C = L_S + 26624;
constexpr u32 LDS_BYTES = 163840;
enum { C_H1 = 0, C_F1 = 1, C_OK = 2  , C_P2A = 6, C_P2B = 7, C_P3A = 8, C_P3B = 9, C_R1 = 10, C_R2 = 11, C_F2 = 12, C_TAG = 14  , C_G = 16  , C_NY = 24  , C_N = 32 };

struct Params {
  const u16 *x, *wr, *wi, *w1, *b1, *w2, *b2, *gw;
  u16* out;
  char* ws;
};

DI u32 pk(float a, float b) { f2v v = {a, b}; return __builtin_bit_cast(u32, __builtin_convertvector(v, b2v)); }
DI float blo(u32 v) { return __uint_as_float(v << 16); }
DI float bhi(u32 v) { return __uint_as_float(v & 0xFFFF0000u); }
DI float rbf(float a) { return blo(pk(a, a)); }
DI float cosr(int n, float inv) { return __builtin_amdgcn_cosf((float)n * inv); }
DI float sinr(int n, float inv) { return __builtin_amdgcn_sinf((float)n * inv); }
DI u32 san(u32 v) { return v < 0xFFFFFFFEu ? v : 0xFFFFFFFEu; }
DI v4u san4(v4u v) { v4u r = {san(v.x), san(v.y), san(v.z), san(v.w)}; return r; }
DI u32 max4(v4u v) { return max(max(v.x, v.y), max(v.z, v.w)); }
DI f4v mfma16(v4u a, v4u b, f4v c) { return __builtin_amdgcn_mfma_f32_16x16x32_bf16(__builtin_bit_cast(s8v, a), __builtin_bit_cast(s8v, b), c, 0, 0, 0); }
DI f16v mfma32(v4u a, v4u b, f16v c) { return __builtin_amdgcn_mfma_f32_32x32x16_bf16(__builtin_bit_cast(s8v, a), __builtin_bit_cast(s8v, b), c, 0, 0, 0); }
DI v2u trd(LAS char* L, u32 off) { return __builtin_bit_cast(v2u, __builtin_amdgcn_ds_read_tr16_b64_v4i16((LAS s4v*)(L + off))); }
DI v4u trd2(LAS char* L, u32 o0, u32 o1) { v2u a = trd(L, o0), b = trd(L, o1); v4u r = {a.x, a.y, b.x, b.y}; return r; }
template <int C> DI float dppadd(float v) { return v + __int_as_float(__builtin_amdgcn_update_dpp(0, __float_as_int(v), C, 0xF, 0xF, true)); }
DI float wsum(float v) {
  v = dppadd<0xB1>(v); v = dppadd<0x4E>(v); v = dppadd<0x141>(v); v = dppadd<0x140>(v);
  auto a = __builtin_amdgcn_permlane16_swap(__float_as_uint(v), __float_as_uint(v), false, false);
  v = __uint_as_float(a[0]) + __uint_as_float(a[1]);
  auto b = __builtin_amdgcn_permlane32_swap(__float_as_uint(v), __float_as_uint(v), false, false);
  return __uint_as_float(b[0]) + __uint_as_float(b[1]);
}
DI u64 mix64(u64 z) { z += 0x9E3779B97F4A7C15ull; z = (z ^ (z >> 30)) * 0xBF58476D1CE4E5B9ull; z = (z ^ (z >> 27)) * 0x94D049BB133111EBull; return z ^ (z >> 31); }
#define WAITL() asm volatile("s_waitcnt lgkmcnt(0)" ::: "memory")
template <int N> DI void waitv() { asm volatile("s_waitcnt vmcnt(%0)" ::"n"(N) : "memory"); }
#define SCHED() __builtin_amdgcn_sched_barrier(0)
DI u32 larrive(LAS char* L, int c, int lane) {
  WAITL();
  u32 old = 0;
  if (lane == 0) old = __hip_atomic_fetch_add((LAS u32*)(L + L_C) + c, 1u, __ATOMIC_RELAXED, __HIP_MEMORY_SCOPE_WORKGROUP);
  return __builtin_amdgcn_readfirstlane(old);
}
template <int SL = 0> DI void lwait(LAS char* L, int c, u32 n) {
  volatile LAS u32* p = (volatile LAS u32*)(L + L_C) + c;
  _Pragma("unroll 1") for (u32 s = 0; (u32)__builtin_amdgcn_readfirstlane(*p) < n && s < 40u * SPIN_CAP; ++s) { if (SL) __builtin_amdgcn_s_sleep(SL); }
  asm volatile("" ::: "memory");
}
DI void lsync(LAS char* L, int c, int lane) { larrive(L, c, lane); lwait(L, c, 4); }

#define STAMP(i)

template <int SGN> DI void dft8(const float (&ar)[8], const float (&ai)[8], float (&yr)[8], float (&yi)[8]) {
  const float R = 0.70710678118654752f, s = (float)SGN;
  float br[8], bi[8];
#pragma unroll
  for (int i = 0; i < 4; ++i) { br[i] = ar[i] + ar[i + 4]; bi[i] = ai[i] + ai[i + 4]; }
  br[4] = ar[0] - ar[4]; bi[4] = ai[0] - ai[4];
  { float r = ar[1] - ar[5], m = ai[1] - ai[5]; br[5] = R * (r - s * m); bi[5] = R * (m + s * r); }
  { float r = ar[2] - ar[6], m = ai[2] - ai[6]; br[6] = -s * m; bi[6] = s * r; }
  { float r = ar[3] - ar[7], m = ai[3] - ai[7]; br[7] = -R * (r + s * m); bi[7] = R * (s * r - m); }
#pragma unroll
  for (int h = 0; h < 2; ++h) {
    const int o = 4 * h;
    float er = br[o] + br[o + 2], ei = bi[o] + bi[o + 2], fr = br[o] - br[o + 2], fi = bi[o] - bi[o + 2];
    float gr = br[o + 1] + br[o + 3], gi = bi[o + 1] + bi[o + 3], hr = br[o + 1] - br[o + 3], hi = bi[o + 1] - bi[o + 3];
    yr[h] = er + gr; yi[h] = ei + gi; yr[h + 4] = er - gr; yi[h + 4] = ei - gi;
    yr[h + 2] = fr - s * hi; yi[h + 2] = fi + s * hr; yr[h + 6] = fr + s * hi; yi[h + 6] = fi - s * hr;
  }
}

struct P2C { v4u f1r, f1i, fr, fi, gr, gi, g1r, g1i; float tc[4], ts[4], uc[4], us[4]; };
DI v4u p2_column(const P2C& c, v4u in, v4u wv) {
  const f4v z = {0.f, 0.f, 0.f, 0.f};
  f4v r = mfma16(in, c.f1r, z), m = mfma16(in, c.f1i, z);
  v4u t;
#pragma unroll
  for (int i = 0; i < 4; ++i) t[i] = pk(r[i] * c.tc[i] + m[i] * c.ts[i], m[i] * c.tc[i] - r[i] * c.ts[i]);
  r = mfma16(c.fr, t, z); m = mfma16(c.fi, t, z);
#pragma unroll
  for (int i = 0; i < 4; ++i) { const float a = blo(wv[i]), b = bhi(wv[i]); t[i] = pk(r[i] * a - m[i] * b, r[i] * b + m[i] * a); }
  r = mfma16(t, c.gr, z); m = mfma16(t, c.gi, z);
#pragma unroll
  for (int i = 0; i < 4; ++i) t[i] = pk(r[i] * c.uc[i] - m[i] * c.us[i], m[i] * c.uc[i] + r[i] * c.us[i]);
  r = mfma16(c.g1r, t, z); m = mfma16(c.g1i, t, z);
#pragma unroll
  for (int i = 0; i < 4; ++i) t[i] = san(pk(r[i], m[i]));
  return t;
}

DI void dma16(u32 ldsaddr, u32 voff, v4i srd, u32 soff) {
  asm volatile("s_mov_b32 m0, %0\n\ts_nop 0\n\tbuffer_load_dwordx4 %1, %2, %3 offen lds" ::"s"(ldsaddr), "v"(voff), "s"(srd), "s"(soff) : "memory");
}
DI void ld16s(v4u& d, u32 voff, v4i srd, u32 soff) { asm volatile("buffer_load_dwordx4 %0, %1, %2, %3 offen sc1" : "=v"(d) : "v"(voff), "s"(srd), "s"(soff) : "memory"); }
DI void ld8s(v2u& d, u32 voff, v4i srd, u32 soff) { asm volatile("buffer_load_dwordx2 %0, %1, %2, %3 offen sc1" : "=v"(d) : "v"(voff), "s"(srd), "s"(soff) : "memory"); }
DI void ld8(v2u& d, u32 voff, v4i srd, u32 soff) { asm volatile("buffer_load_dwordx2 %0, %1, %2, %3 offen" : "=v"(d) : "v"(voff), "s"(srd), "s"(soff) : "memory"); }
DI void st16s(v4u d, u32 voff, v4i srd, u32 soff) { asm volatile("buffer_store_dwordx4 %0, %1, %2, %3 offen sc1\n\ts_nop 1" ::"v"(d), "v"(voff), "s"(srd), "s"(soff) : "memory"); }
DI void st8s(v2u d, u32 voff, v4i srd, u32 soff) { asm volatile("buffer_store_dwordx2 %0, %1, %2, %3 offen sc1\n\ts_nop 1" ::"v"(d), "v"(voff), "s"(srd), "s"(soff) : "memory"); }
template <int N> DI void waitv2(v4u& a, v4u& b) { asm volatile("s_waitcnt vmcnt(%2)" : "+v"(a), "+v"(b) : "n"(N) : "memory"); }
DI v4i mksrd(const void* p, u32 bytes) { const u64 a = (u64)p; v4i r = {(int)(u32)a, (int)((u32)(a >> 32) & 0xFFFFu), (int)bytes, 0x00020000}; return r; }

constexpr int RING = 6;
constexpr int g1_extra(int k) { return (k == 3 ? 8 : 0) + (k == 25 ? 4 : 0) + (k >= 26 && k <= 29 ? 4 : 0) + (k == 30 ? 4 : 0); }
constexpr int g1_after(int k) {
  int n = 0;
  if (k < RING) n += 2 * (RING - 1 - k);
  else n += g1_extra(k - RING);
  for (int j = (k < RING ? 0 : k - RING + 1); j < k; ++j) n += (j + RING < 32 ? 2 : 0) + g1_extra(j);
  return n;
}
constexpr int g2_extra(int k) { return k == 22 ? 3 : 0; }
constexpr int g2_after(int k) {
  int n = 0;
  if (k < RING) n += 2 * (RING - 1 - k);
  else n += g2_extra(k - RING);
  for (int j = (k < RING ? 0 : k - RING + 1); j < k; ++j) n += (j + RING < 28 ? 2 : 0) + g2_extra(j);
  return n;
}

template <int K> struct IC { static constexpr int v = K; };
template <int B, int E, class Fn> DI void sfor(Fn&& f) { if constexpr (B < E) { f(IC<B>{}); sfor<B + 1, E>(f); } }

__global__ void __launch_bounds__(512, 2) fused_kernel(Params p) {
  extern __shared__ __attribute__((aligned(16))) char smem[];
  LAS char* L = (LAS char*)smem;
  const int tid = threadIdx.x, lane = tid & 63, wv = __builtin_amdgcn_readfirstlane(tid >> 6), w = wv & 3;
  const int b = blockIdx.x, xl = b & 7, idx = b >> 3;
  const int dtl = idx & 3, dt = 4 * (xl >> 2) + dtl, grp = xl & 3;
  const int st = 8 * grp + (idx >> 2);
  STAMP(0);
  const rsrc_t rw = __builtin_amdgcn_make_buffer_rsrc((void*)p.ws, 0, (int)WS_BYTES, 0x00020000);
  asm volatile("" ::"s"(p.x), "s"(p.wr), "s"(p.wi), "s"(p.w1), "s"(p.b1), "s"(p.w2), "s"(p.b2), "s"(p.gw), "s"(p.out), "s"(p.ws));
  v4u fin_ga; u32 fin_om, fin_b2;

  if (wv < 4) {
    __builtin_amdgcn_s_setprio(3);
    const int s0 = b, q = lane >> 4, c = lane & 15;
    const rsrc_t rx = __builtin_amdgcn_make_buffer_rsrc((void*)p.x, 0, 2048 * 512 * 2, 0x00020000);
    const v4u x0 = __builtin_amdgcn_raw_buffer_load_b128(rx, lane * 16, (s0 + 512 * w) * 1024, 0);
    const v4u x1 = __builtin_amdgcn_raw_buffer_load_b128(rx, lane * 16, (s0 + 512 * w + 256) * 1024, 0);
    const v4u gwv = *(const v4u*)(p.gw + lane * 8);
    const v2u epv = __builtin_amdgcn_raw_buffer_load_b64(rw, O_EPOCH, 0, 0);
    {
      const v4u P = {POISON, POISON, POISON, POISON};
      __builtin_amdgcn_raw_buffer_store_b128(P, rw, lane * 16, O_X1 + (2 * w) * 262144 + s0 * 1024, AUX_SC1);
      __builtin_amdgcn_raw_buffer_store_b128(P, rw, lane * 16, O_X1 + (2 * w + 1) * 262144 + s0 * 1024, AUX_SC1);
      __builtin_amdgcn_raw_buffer_store_b128(P, rw, lane * 16, O_V + b * 8192 + w * 2048, AUX_SC1);
      __builtin_amdgcn_raw_buffer_store_b128(P, rw, lane * 16, O_V + b * 8192 + w * 2048 + 1024, AUX_SC1);
      __builtin_amdgcn_raw_buffer_store_b128(P, rw, lane * 16, O_GA + b * 8192 + w * 2048, AUX_SC1);
      __builtin_amdgcn_raw_buffer_store_b128(P, rw, lane * 16, O_GA + b * 8192 + w * 2048 + 1024, AUX_SC1);
      if (w == 0 && lane < 24)
        __builtin_amdgcn_raw_buffer_store_b32(POISON, rw, lane < 8 ? O_XN + lane * 1024 + s0 * 4 : (lane < 16 ? O_VN : O_OMG) + b * 32 + (lane & 7) * 4, 0, AUX_SC1);
    }
    if (tid < C_N) ((LAS u32*)(L + L_C))[tid] = 0;
    WAITL();
    __builtin_amdgcn_s_barrier();
    float xa[8], xb[8];
#pragma unroll
    for (int i = 0; i < 4; ++i) { xa[2 * i] = blo(x0[i]); xa[2 * i + 1] = bhi(x0[i]); xb[2 * i] = blo(x1[i]); xb[2 * i + 1] = bhi(x1[i]); }
    STAMP(1);
    *(LAS v4u*)(L + L_X + (2 * w) * 1088 + lane * 16) = x0;
    *(LAS v4u*)(L + L_X + (2 * w + 1) * 1088 + lane * 16) = x1;
    {
      float sa = ((xa[0] + xa[1]) + (xa[2] + xa[3])) + ((xa[4] + xa[5]) + (xa[6] + xa[7]));
      float sb = ((xb[0] + xb[1]) + (xb[2] + xb[3])) + ((xb[4] + xb[5]) + (xb[6] + xb[7]));
      const float ma = wsum(sa) * (1.f / 512.f), mb = wsum(sb) * (1.f / 512.f);
      float va = 0.f, vb = 0.f;
#pragma unroll
      for (int i = 0; i < 8; ++i) { const float da = xa[i] - ma, db = xb[i] - mb; va += da * da; vb += db * db; }
      va = wsum(va) * (1.f / 512.f); vb = wsum(vb) * (1.f / 512.f);
      const float ra = __builtin_amdgcn_rcpf(rbf(__builtin_amdgcn_sqrtf(rbf(rbf(va) + 1e-5f))));
      const float rb = __builtin_amdgcn_rcpf(rbf(__builtin_amdgcn_sqrtf(rbf(rbf(vb) + 1e-5f))));
      const float na = -rbf(ma) * ra, nb = -rbf(mb) * rb;
      v4u ha, hb;
#pragma unroll
      for (int i = 0; i < 4; ++i) {
        ha[i] = pk(xa[2 * i] * ra + na, xa[2 * i + 1] * ra + na);
        hb[i] = pk(xb[2 * i] * rb + nb, xb[2 * i + 1] * rb + nb);
      }
      *(LAS v4u*)(L + L_HR + (2 * w) * 1088 + lane * 16) = ha;
      *(LAS v4u*)(L + L_HR + (2 * w + 1) * 1088 + lane * 16) = hb;
    }
    lsync(L, C_H1, lane);
    STAMP(11);
    {
      const u32 ta = L_HR + (c >> 2) * 1088 + (128 * w + 16 * q + 4 * (c & 3)) * 2;
      const v4u h0 = trd2(L, ta, ta + 4 * 1088), h1 = trd2(L, ta + 128, ta + 128 + 4 * 1088);
      const u32 vo = (lane >> 5) * 131072 + (lane & 31) * 16;
      __builtin_amdgcn_raw_buffer_store_b128(h0, rw, vo, O_H + (4 * w) * 131072 + s0 * 512, AUX_SC1);
      __builtin_amdgcn_raw_buffer_store_b128(h1, rw, vo, O_H + (4 * w + 2) * 131072 + s0 * 512, AUX_SC1);
    }
    STAMP(2);
    float om_keep;
    {
      float ga = 0.f, gb = 0.f;
#pragma unroll
      for (int i = 0; i < 4; ++i) {
        const float g0 = blo(gwv[i]), g1 = bhi(gwv[i]);
        const u32 pa = pk(xa[2 * i] * g0, xa[2 * i + 1] * g1), pb = pk(xb[2 * i] * g0, xb[2 * i + 1] * g1);
        ga += blo(pa) + bhi(pa); gb += blo(pb) + bhi(pb);
      }
      ga = rbf(wsum(ga)); gb = rbf(wsum(gb));
      const float gs = (lane & 1) ? gb : ga;
      const float g = rbf(__builtin_amdgcn_rcpf(1.f + __builtin_amdgcn_exp2f(-1.44269504088896f * gs)));
      om_keep = rbf(1.f - g);
      if (lane < 2) ((LAS float*)(L + L_C))[C_G + 2 * w + lane] = g;
    }
    v4u f64r[2], f64i[2], a8;
    float twc[4], tws[4];
    {
      const int k1 = 16 * w + c;
#pragma unroll
      for (int ks = 0; ks < 2; ++ks)
#pragma unroll
        for (int j = 0; j < 4; ++j) {
          const int a = 32 * ks + 8 * q + 2 * j, n0 = (a * k1) & 63, n1 = ((a + 1) * k1) & 63;
          f64r[ks][j] = pk(cosr(n0, 1.f / 64), cosr(n1, 1.f / 64));
          f64i[ks][j] = pk(-sinr(n0, 1.f / 64), -sinr(n1, 1.f / 64));
        }
#pragma unroll
      for (int r = 0; r < 4; ++r) {
        const int bb = 4 * (q & 1) + r, n = (bb * (c >> 2)) & 7;
        const float cs = cosr(n, 0.125f), sn = sinr(n, 0.125f);
        const u32 e = (c & 1) ? pk(-sn, cs) : pk(cs, sn);
        a8[r] = (((c >> 1) & 1) == (q >> 1)) ? e : 0u;
        twc[r] = cosr(bb * k1, 1.f / 512); tws[r] = sinr(bb * k1, 1.f / 512);
      }
    }
    const u64 tag = mix64(((u64)(u32)__builtin_amdgcn_readfirstlane(epv.y) << 32) | (u32)__builtin_amdgcn_readfirstlane(epv.x));
    if (lane == 0) { const v2u tg = {(u32)tag, (u32)(tag >> 32)}; *(LAS v2u*)(L + L_C + C_TAG * 4) = tg; }
    STAMP(12);
    waitv<0>();
    STAMP(3);
    if (larrive(L, C_F1, lane) == 3 && lane < 8) {
      const v2u tg = {(u32)tag, (u32)(tag >> 32)};
      __builtin_amdgcn_raw_buffer_store_b64(tg, rw, O_F1 + lane * 2048 + b * 8, 0, AUX_SC1);
    }
    const int j1 = xl, kb = idx;
    v4u wq0, wq1;
    {
      const rsrc_t rr = __builtin_amdgcn_make_buffer_rsrc((void*)p.wr, 0, 2048 * 257 * 2, 0x00020000);
      const rsrc_t ri = __builtin_amdgcn_make_buffer_rsrc((void*)p.wi, 0, 2048 * 257 * 2, 0x00020000);
      const u32 o = (u32)(j1 + 8 * (64 * w + lane)) * 514u + kb * 16;
      wq0 = __builtin_amdgcn_raw_buffer_load_b128(rr, o, 0, 0);
      wq1 = __builtin_amdgcn_raw_buffer_load_b128(ri, o, 0, 0);
    }
    {
      float yr[8], yi[8], nr[8], ni[8];
      const f4v z = {0.f, 0.f, 0.f, 0.f};
      const u32 xo = L_X + ((c >> 1) & 1) * 1088 + (8 * q + (c >> 2)) * 16 + 8 * (c & 1);
      v4u a8n = a8; a8n[1] ^= 0x80008000u; a8n[3] ^= 0x80008000u;
#pragma unroll
      for (int pi = 0; pi < 4; ++pi) {
        const v4u A0 = trd2(L, xo + pi * 2176, xo + pi * 2176 + 64), A1 = trd2(L, xo + pi * 2176 + 512, xo + pi * 2176 + 576);
        f4v r = mfma16(A0, f64r[0], z), m = mfma16(A0, f64i[0], z);
        r = mfma16(A1, f64r[1], r); m = mfma16(A1, f64i[1], m);
        v4u t;
#pragma unroll
        for (int i = 0; i < 4; ++i) t[i] = pk(r[i] * twc[i] + m[i] * tws[i], m[i] * twc[i] - r[i] * tws[i]);
        const f4v d = mfma16(a8, t, z);
        yr[2 * pi] = d[0]; yi[2 * pi] = d[1]; yr[2 * pi + 1] = d[2]; yi[2 * pi + 1] = d[3];
        if (w == 0) { const f4v e = mfma16(a8n, t, z); nr[2 * pi] = e[0]; ni[2 * pi] = e[1]; nr[2 * pi + 1] = e[2]; ni[2 * pi + 1] = e[3]; }
      }
      float zr[8], zi[8];
      dft8<-1>(yr, yi, zr, zi);
#pragma unroll
      for (int j = 0; j < 8; ++j) *(LAS u32*)(L + L_T + w * 2048 + j * 256 + lane * 4) = san(pk(zr[j], zi[j]));
      const v4u o0 = *(LAS v4u*)(L + L_T + w * 2048 + lane * 16), o1 = *(LAS v4u*)(L + L_T + w * 2048 + 1024 + lane * 16);
      STAMP(13);
      lwait(L, C_F1, 4);
      const u32 vo = q * 262144 + (((lane >> 2) & 3) * 64 + (lane & 3) * 4) * 4;
      __builtin_amdgcn_raw_buffer_store_b128(o0, rw, vo, O_X1 + s0 * 1024 + w * 64, AUX_SC1);
      __builtin_amdgcn_raw_buffer_store_b128(o1, rw, vo, O_X1 + 4 * 262144 + s0 * 1024 + w * 64, AUX_SC1);
      if (w == 0) {
        dft8<-1>(nr, ni, zr, zi);
        if (lane == 0) {
#pragma unroll
          for (int j = 0; j < 8; ++j) __builtin_amdgcn_raw_buffer_store_b32(san(pk(zr[j], zi[j])), rw, O_XN + j * 1024 + s0 * 4, 0, AUX_SC1);
        }
      }
    }
    STAMP(4);
    const u32 epos = ((w * 16 + c) * 4 + q) * 4;
    P2C pc;
    {
#pragma unroll
      for (int i = 0; i < 4; ++i) {
        const int e = 4 * q + i;
        { const int n = (e * (8 * c + j1)) & 127; const float cs = cosr(n, 1.f / 128), sn = sinr(n, 1.f / 128); pc.f1r[i] = pk(cs, sn); pc.f1i[i] = pk(-sn, cs); }
        { const int n = (e * c) & 15; const float cs = cosr(n, 1.f / 16), sn = sinr(n, 1.f / 16); pc.fr[i] = pk(cs, sn); pc.fi[i] = pk(-sn, cs); pc.gr[i] = pk(cs, -sn); pc.gi[i] = pk(sn, cs); }
        { const int n = (c * (8 * e + j1)) & 127; const float cs = cosr(n, 1.f / 128), sn = sinr(n, 1.f / 128); pc.g1r[i] = pk(cs, -sn); pc.g1i[i] = pk(sn, cs); }
        pc.tc[i] = cosr(e * (8 * c + j1), 1.f / 2048); pc.ts[i] = sinr(e * (8 * c + j1), 1.f / 2048);
        pc.uc[i] = cosr(c * (8 * e + j1), 1.f / 2048); pc.us[i] = sinr(c * (8 * e + j1), 1.f / 2048);
      }
#pragma unroll
      for (int i = 0; i < 4; ++i) {
        u32 lo = __builtin_amdgcn_perm(wq1[i], wq0[i], 0x05040100u), hi = __builtin_amdgcn_perm(wq1[i], wq0[i], 0x07060302u);
        if (i == 0 && kb == 0) lo = pk(0.5f * blo(lo), 0.5f * bhi(lo));
        *(LAS u32*)(L + L_W + (2 * i) * 1024 + epos) = lo;
        *(LAS u32*)(L + L_W + (2 * i + 1) * 1024 + epos) = hi;
      }
    }
    {
      const u32 fo = O_F1 + xl * 2048 + (64 * w + lane) * 8;
      _Pragma("unroll 1") for (u32 s = 0; s < SPIN_CAP; ++s) {
        asm volatile("" ::: "memory");
        const v2u f = __builtin_amdgcn_raw_buffer_load_b64(rw, fo, 0, AUX_SC1);
        if (__all(f.x == (u32)tag && f.y == (u32)(tag >> 32))) break;
        __builtin_amdgcn_s_sleep(2);
      }
      asm volatile("" ::: "memory");
      if (lane == 0) { ((volatile LAS u32*)(L + L_C))[C_OK + w] = 1u; }
      STAMP(5);
      lwait(L, C_OK + 0, 1); lwait(L, C_OK + 1, 1); lwait(L, C_OK + 2, 1); lwait(L, C_OK + 3, 1);
    }
    if (b == 0 && tid == 0) *(u64*)(p.ws + O_EPOCH) = tag;
    if (lane < 2) __builtin_amdgcn_raw_buffer_store_b32(__float_as_uint(om_keep), rw, O_OMG + (s0 + 256 * (2 * w + lane)) * 4, 0, AUX_SC1);
    {
      const u32 xo = O_X1 + j1 * 262144 + (64 * w + lane) * 1024 + kb * 32;
      v4u e0 = __builtin_amdgcn_raw_buffer_load_b128(rw, xo, 0, AUX_SC1), e1 = __builtin_amdgcn_raw_buffer_load_b128(rw, xo + 16, 0, AUX_SC1);
      _Pragma("unroll 1") for (u32 s = 0; s < SPIN_CAP; ++s) {
        asm volatile("" ::: "memory");
        const bool bad = max(max4(e0), max4(e1)) == POISON;
        if (!__any(bad)) break;
        __builtin_amdgcn_s_sleep(1);
        if (bad) { e0 = __builtin_amdgcn_raw_buffer_load_b128(rw, xo, 0, AUX_SC1); e1 = __builtin_amdgcn_raw_buffer_load_b128(rw, xo + 16, 0, AUX_SC1); }
      }
      STAMP(6);
#pragma unroll
      for (int i = 0; i < 4; ++i) { *(LAS u32*)(L + L_E + i * 1024 + epos) = e0[i]; *(LAS u32*)(L + L_E + (4 + i) * 1024 + epos) = e1[i]; }
      lsync(L, C_P2A, lane);
      const v4u i0 = *(LAS v4u*)(L + L_E + (2 * w) * 1024 + lane * 16), i1 = *(LAS v4u*)(L + L_E + (2 * w + 1) * 1024 + lane * 16);
      const v4u w0 = *(LAS v4u*)(L + L_W + (2 * w) * 1024 + lane * 16), w1 = *(LAS v4u*)(L + L_W + (2 * w + 1) * 1024 + lane * 16);
      *(LAS v4u*)(L + L_O + (2 * w) * 1024 + lane * 16) = p2_column(pc, i0, w0);
      *(LAS v4u*)(L + L_O + (2 * w + 1) * 1024 + lane * 16) = p2_column(pc, i1, w1);
      lsync(L, C_P2B, lane);
      v4u o0, o1;
#pragma unroll
      for (int i = 0; i < 4; ++i) { o0[i] = *(LAS u32*)(L + L_O + i * 1024 + epos); o1[i] = *(LAS u32*)(L + L_O + (4 + i) * 1024 + epos); }
      STAMP(14);
      const u32 vo = O_V + (64 * w + lane) * 8192 + j1 * 1024 + kb * 32;
      __builtin_amdgcn_raw_buffer_store_b128(o0, rw, vo, 0, AUX_SC1);
      __builtin_amdgcn_raw_buffer_store_b128(o1, rw, vo + 16, 0, AUX_SC1);
      if (kb == 31 && w == 0) {
        v4u in, wn;
#pragma unroll
        for (int i = 0; i < 4; ++i) {
          const u32 e = 64 * q + 16 * i + c, o = (j1 + 8 * e) * 257 + 256;
          wn[i] = pk(0.5f * blo(p.wr[o]), 0.5f * blo(p.wi[o]));
          in[i] = POISON;
        }
        _Pragma("unroll 1") for (u32 s = 0; s < SPIN_CAP; ++s) {
        asm volatile("" ::: "memory");
#pragma unroll
          for (int i = 0; i < 4; ++i) in[i] = __builtin_amdgcn_raw_buffer_load_b32(rw, O_XN + j1 * 1024 + (64 * q + 16 * i + c) * 4, 0, AUX_SC1);
          if (!__any(max4(in) == POISON)) break;
        }
        const v4u o = p2_column(pc, in, wn);
#pragma unroll
        for (int i = 0; i < 4; ++i) __builtin_amdgcn_raw_buffer_store_b32(o[i], rw, O_VN + (64 * q + 16 * i + c) * 32 + j1 * 4, 0, AUX_SC1);
      }
    }
    STAMP(7);
    {
      v4u gB[4];
      float pcs[2], psn[2];
      u32 ax;
      {
        const int ap = 16 * w + c;
#pragma unroll
        for (int ks = 0; ks < 4; ++ks)
#pragma unroll
          for (int i = 0; i < 4; ++i) {
            const int n = ((16 * ks + 4 * q + i) * ap) & 63;
            gB[ks][i] = pk(cosr(n, 1.f / 64) * 0x1p-19f, -sinr(n, 1.f / 64) * 0x1p-19f);
          }
        const int n = (q * (c >> 1)) & 7;
        const float cs = cosr(n, 0.125f), sn = sinr(n, 0.125f);
        ax = (c & 1) ? pk(sn, cs) : pk(cs, -sn);
#pragma unroll
        for (int e = 0; e < 2; ++e) { pcs[e] = cosr(ap * (2 * q + e), 1.f / 512); psn[e] = sinr(ap * (2 * q + e), 1.f / 512); }
      }
      const u32 vo = O_V + s0 * 8192 + (16 * w + 64 * q + c) * 4;
      u32 vv[8];
#pragma unroll
      for (int j = 0; j < 8; ++j) vv[j] = __builtin_amdgcn_raw_buffer_load_b32(rw, vo + j * 1024, 0, AUX_SC1);
      _Pragma("unroll 1") for (u32 s = 0; s < SPIN_CAP; ++s) {
        asm volatile("" ::: "memory");
        const bool bad = max(max(max(vv[0], vv[1]), max(vv[2], vv[3])), max(max(vv[4], vv[5]), max(vv[6], vv[7]))) == POISON;
        if (!__any(bad)) break;
        __builtin_amdgcn_s_sleep(1);
        if (bad) {
#pragma unroll
          for (int j = 0; j < 8; ++j) vv[j] = __builtin_amdgcn_raw_buffer_load_b32(rw, vo + j * 1024, 0, AUX_SC1);
        }
      }
      STAMP(8);
      float ar[8], ai[8], yr[8], yi[8];
#pragma unroll
      for (int j = 0; j < 8; ++j) { ar[j] = blo(vv[j]); ai[j] = bhi(vv[j]); }
      dft8<1>(ar, ai, yr, yi);
      v4u yq[2];
#pragma unroll
      for (int i = 0; i < 8; ++i) yq[i >> 2][i & 3] = pk(yr[i], yi[i]);
      const f4v z = {0.f, 0.f, 0.f, 0.f};
#pragma unroll
      for (int i = 0; i < 8; ++i) {
        v4u A = {0u, 0u, 0u, 0u};
        A[i & 3] = ax;
        const f4v d = mfma16(A, yq[i >> 2], z);
        *(LAS u32*)(L + L_U + i * 2176 + (2 * q) * 272 + (16 * w + c) * 4) = pk(d[0] * pcs[0] - d[1] * psn[0], d[1] * pcs[0] + d[0] * psn[0]);
        *(LAS u32*)(L + L_U + i * 2176 + (2 * q + 1) * 272 + (16 * w + c) * 4) = pk(d[2] * pcs[1] - d[3] * psn[1], d[3] * pcs[1] + d[2] * psn[1]);
      }
      if (w == 0) {
        v4u n0, n1;
        _Pragma("unroll 1") for (u32 s = 0; s < SPIN_CAP; ++s) {
        asm volatile("" ::: "memory");
          n0 = __builtin_amdgcn_raw_buffer_load_b128(rw, O_VN + s0 * 32, 0, AUX_SC1);
          n1 = __builtin_amdgcn_raw_buffer_load_b128(rw, O_VN + s0 * 32 + 16, 0, AUX_SC1);
          if (!__any(max(max4(n0), max4(n1)) == POISON)) break;
        }
#pragma unroll
        for (int j = 0; j < 4; ++j) { ar[j] = blo(n0[j]); ai[j] = bhi(n0[j]); ar[j + 4] = blo(n1[j]); ai[j + 4] = bhi(n1[j]); }
        dft8<1>(ar, ai, yr, yi);
        if (lane == 0) {
#pragma unroll
          for (int i = 0; i < 8; ++i) ((LAS float*)(L + L_C))[C_NY + i] = yr[i] * 0x1p-19f;
        }
      }
      lsync(L, C_P3B, lane);
      const u32 uo = L_U + (c >> 3) * 2176 + (c & 7) * 272 + q * 16;
#pragma unroll
      for (int pi = 0; pi < 4; ++pi) {
        f4v d = z;
#pragma unroll
        for (int ks = 0; ks < 4; ++ks) d = mfma16(*(LAS v4u*)(L + uo + pi * 4352 + ks * 64), gB[ks], d);
        const int i = 2 * pi + (q >> 1);
        const float ny = ((LAS float*)(L + L_C))[C_NY + i], g = ((LAS float*)(L + L_C))[C_G + i];
        const u32 a01 = pk(d[0] + ny, d[1] - ny), a23 = pk(d[2] + ny, d[3] - ny);
        const v2u o = {san(pk(g * blo(a01), g * bhi(a01))), san(pk(g * blo(a23), g * bhi(a23)))};
        __builtin_amdgcn_raw_buffer_store_b64(o, rw, O_GA + (s0 + 256 * i) * 1024 + ((16 * w + c) * 8 + 4 * (q & 1)) * 2, 0, AUX_SC1);
      }
    }
    STAMP(9);
    {
      const int sl = (tid >> 3) & 63, s = 64 * st + sl;
      fin_b2 = p.b2[s];
      const u32 go = O_GA + s * 1024 + dt * 128 + (tid & 7) * 16;
      _Pragma("unroll 1") for (u32 sp = 0; sp < SPIN_CAP; ++sp) {
        asm volatile("" ::: "memory");
        fin_ga = __builtin_amdgcn_raw_buffer_load_b128(rw, go, 0, AUX_SC1);
        fin_om = __builtin_amdgcn_raw_buffer_load_b32(rw, O_OMG + s * 4, 0, AUX_SC1);
        if (!__any(max(max4(fin_ga), fin_om) == POISON)) break;
        __builtin_amdgcn_s_sleep(4);
      }
    }
    STAMP(10);
  } else {
    __builtin_amdgcn_s_barrier();
    const v4i sw = mksrd(p.ws, WS_BYTES), s2 = mksrd(p.w2, 1792u * 4096u);
    u32 lb = (u32)(size_t)smem + L_G + w * 32768, w2b = (u32)(448 * w) * 4096u + st * 128, rot2 = 7 * dtl;
    const u32 lo = L_G + w * 32768;
    const int hh = lane >> 5, blk = (lane >> 4) & 1, tq = (lane & 15) >> 2, tp = lane & 3;
    const u32 v2off = (lane >> 3) * 4096 + (((lane & 7) ^ (((lane >> 4) & 1) << 2)) * 16);
    auto w2dma = [&](int kl, u32 dst) {
      u32 kg = kl + rot2; kg = kg >= 28 ? kg - 28 : kg;
      const u32 so = w2b + kg * 65536u;
      dma16(lb + dst, v2off, s2, so);
      dma16(lb + dst + 1024, v2off, s2, so + 8 * 4096);
    };
    const u32 t2 = lo + (4 * hh + tq) * 128 + (((2 * blk + (tp >> 1)) ^ (((tq >> 1) & 1) << 2)) * 16) + 8 * (tp & 1);
    v4u wr_[24];
    f16v acc[4];
    v4u ring[2 * RING];
    if (idx < 28) {
      const int ft = 7 * grp + (idx >> 2);
      const v4i s1 = mksrd(p.w1, 2048u * 3584u);
      u32 v1off[4];
#pragma unroll
      for (int j = 0; j < 4; ++j) v1off[j] = ((lane >> 3) ^ (j >> 1)) * 3584 + ((lane & 7) ^ ((j & 1) << 2)) * 16;
      auto w1dma = [&](int bl, int slot) {
        const u32 so = (u32)(64 * w + 8 * ((bl + 2 * dtl) & 7)) * 3584u + ft * 128;
#pragma unroll
        for (int j = 0; j < 8; ++j) dma16(lb + slot * 8192 + j * 1024, v1off[j & 3], s1, so + j * (256u * 3584u));
      };
      const u32 t1 = lo + tq * 1024 + ((hh ^ (tq >> 1)) * 128) + ((((w >> 1) * 4 + 2 * blk + (tp >> 1)) ^ ((tq & 1) << 2)) * 16) + 8 * (tp & 1);
      const u32 t1x = t1 ^ 64u;
      w1dma(0, 0); w1dma(1, 1); STAMP(13); w1dma(2, 2); w1dma(3, 3);
      STAMP(1);
      sfor<0, 3>([&](auto B) {
        constexpr int bl = decltype(B)::v;
        waitv<24>();
#pragma unroll
        for (int k = 0; k < 4; ++k) {
          wr_[8 * bl + 2 * k] = trd2(L, t1 + bl * 8192 + k * 256, t1 + bl * 8192 + k * 256 + 4096);
          wr_[8 * bl + 2 * k + 1] = trd2(L, t1x + bl * 8192 + k * 256, t1x + bl * 8192 + k * 256 + 4096);
        }
        WAITL();
        w1dma(4 + bl, bl);
      });
      STAMP(2);
      lwait<1>(L, C_OK + w, 1);
      STAMP(3);
      auto hload = [&](int k) {
        const int pos = k >> 2, bl = pos < 4 ? pos + 3 : (pos < 7 ? pos - 4 : 7);
        const u32 so = O_H + (u32)(64 * w + 8 * ((bl + 2 * dtl) & 7) + 2 * (k & 3)) * 512u;
        ld16s(ring[2 * (k % RING)], lane * 16, sw, so + (2 * dt + (w & 1)) * 131072);
        ld16s(ring[2 * (k % RING) + 1], lane * 16, sw, so + (2 * dt + ((w & 1) ^ 1)) * 131072);
      };
#pragma unroll
      for (int k = 0; k < RING; ++k) hload(k);
#pragma unroll
      for (int i = 0; i < 4; ++i)
#pragma unroll
        for (int j = 0; j < 16; ++j) acc[i][j] = 0.f;
      v2u bias[4];
      v4u wf[2][2];
      sfor<0, 32>([&](auto K) {
        constexpr int k = decltype(K)::v, pos = k >> 2;
        waitv2<g1_after(k)>(ring[2 * (k % RING)], ring[2 * (k % RING) + 1]);
        if constexpr (k == 0) {
          STAMP(4);
          wf[0][0] = trd2(L, t1 + 3 * 8192, t1 + 3 * 8192 + 4096); wf[0][1] = trd2(L, t1x + 3 * 8192, t1x + 3 * 8192 + 4096);
        }
        constexpr int kn = k + 1, pn = kn >> 2;
        if constexpr (kn < 32 && (pn < 4 || pn == 7)) {
          constexpr u32 o = (pn == 0 || pn == 7 ? 3 : pn - 1) * 8192 + (kn & 3) * 256;
          wf[kn & 1][0] = trd2(L, t1 + o, t1 + o + 4096); wf[kn & 1][1] = trd2(L, t1x + o, t1x + o + 4096);
        }
        const v4u a0 = (pos >= 4 && pos < 7) ? wr_[2 * ((k - 16) & 15) % 24] : wf[k & 1][0];
        const v4u a1 = (pos >= 4 && pos < 7) ? wr_[(2 * ((k - 16) & 15) + 1) % 24] : wf[k & 1][1];
        acc[0] = mfma32(a0, ring[2 * (k % RING)], acc[0]);
        acc[1] = mfma32(a0, ring[2 * (k % RING) + 1], acc[1]);
        acc[2] = mfma32(a1, ring[2 * (k % RING)], acc[2]);
        acc[3] = mfma32(a1, ring[2 * (k % RING) + 1], acc[3]);
        SCHED();
        if constexpr (k + RING < 32) hload(k + RING);
        if constexpr (k == 15) { STAMP(14); }
        if constexpr (k == 3) { WAITL(); w1dma(7, 3); }
        if constexpr (k == 25) {
          const v4i sb = mksrd(p.b1, 1792 * 2);
#pragma unroll
          for (int g = 0; g < 4; ++g) ld8(bias[g], (8 * g + 4 * hh) * 2, sb, (64 * ft + 32 * (w >> 1)) * 2);
        }
        if constexpr (k >= 26 && k <= 30) { w2dma(2 * (k - 26), 2 * (k - 26) * 2048); w2dma(2 * (k - 26) + 1, (2 * (k - 26) + 1) * 2048); }
        SCHED();
      });
      STAMP(5);
#pragma unroll
      for (int l = 1; l < 4; ++l)
#pragma unroll
        for (int g = 0; g < 4; ++g) {
          const f4v v = {acc[l][4 * g], acc[l][4 * g + 1], acc[l][4 * g + 2], acc[l][4 * g + 3]};
          *(LAS f4v*)(L + lo + 20480 + (l - 1) * 4096 + g * 1024 + lane * 16) = v;
        }
      lsync(L, C_R1, lane);
#pragma unroll
      for (int l = 1; l < 4; ++l)
#pragma unroll
        for (int g = 0; g < 4; ++g) {
          const f4v v = *(LAS f4v*)(L + L_G + (w ^ l) * 32768 + 20480 + (l - 1) * 4096 + g * 1024 + lane * 16);
#pragma unroll
          for (int e = 0; e < 4; ++e) acc[0][4 * g + e] += v[e];
        }
      larrive(L, C_R2, lane);
      asm volatile("s_waitcnt vmcnt(20)" : "+v"(bias[0]), "+v"(bias[1]), "+v"(bias[2]), "+v"(bias[3])::"memory");
      v4u uf[2];
#pragma unroll
      for (int g = 0; g < 4; ++g) {
        float o[4];
#pragma unroll
        for (int e = 0; e < 4; ++e) {
          const float v = acc[0][4 * g + e] + ((e & 1) ? bhi(bias[g][e >> 1]) : blo(bias[g][e >> 1]));
          const float t = v * (-2.3022081985f - 0.1029432397f * v * v);
          o[e] = v * __builtin_amdgcn_rcpf(1.f + __builtin_amdgcn_exp2f(t));
        }
        uf[g >> 1][2 * (g & 1)] = pk(o[0], o[1]); uf[g >> 1][2 * (g & 1) + 1] = pk(o[2], o[3]);
      }
      const u32 uo = O_U + (u32)((2 * dt + (w & 1)) * 112 + 4 * ft + 2 * (w >> 1)) * 1024u;
      st16s(uf[0], lane * 16, sw, uo);
      st16s(uf[1], lane * 16, sw, uo + 1024);
      STAMP(6);
      waitv<2>();
#pragma unroll
      for (int k = 0; k < 10; ++k) { wr_[2 * k] = trd2(L, t2 + k * 2048, t2 + k * 2048 + 1024); wr_[2 * k + 1] = trd2(L, (t2 ^ 64u) + k * 2048, (t2 ^ 64u) + k * 2048 + 1024); }
      WAITL();
#pragma unroll
      for (int k = 16; k < 20; ++k) w2dma(k, (k - 16) * 2048);
      waitv<8>();
      if (larrive(L, C_F2, lane) == 3 && lane == 0) st8s(*(LAS v2u*)(L + L_C + C_TAG * 4), 0, sw, O_F2 + (dt * 28 + ft) * 128);
      STAMP(7);
      lwait(L, C_R2, 4);
#pragma unroll
      for (int k = 10; k < 16; ++k) w2dma(k, k * 2048);
#pragma unroll
      for (int k = 20; k < 26; ++k) w2dma(k, (k - 16) * 2048);
      waitv<20>();
    } else {
#pragma unroll
      for (int k = 0; k < 16; ++k) w2dma(k, k * 2048);
      waitv<12>();
#pragma unroll
      for (int k = 0; k < 10; ++k) { wr_[2 * k] = trd2(L, t2 + k * 2048, t2 + k * 2048 + 1024); wr_[2 * k + 1] = trd2(L, (t2 ^ 64u) + k * 2048, (t2 ^ 64u) + k * 2048 + 1024); }
      WAITL();
#pragma unroll
      for (int k = 16; k < 26; ++k) w2dma(k, (k - 16) * 2048);
      waitv<20>();
    }
    asm volatile("" : "+s"(lb), "+s"(w2b), "+s"(rot2));
#pragma unroll
    for (int k = 10; k < 12; ++k) { wr_[2 * k] = trd2(L, t2 + k * 2048, t2 + k * 2048 + 1024); wr_[2 * k + 1] = trd2(L, (t2 ^ 64u) + k * 2048, (t2 ^ 64u) + k * 2048 + 1024); }
    WAITL();
    w2dma(26, 10 * 2048); w2dma(27, 11 * 2048);
    lwait<1>(L, C_OK + 0, 1); lwait<1>(L, C_OK + 1, 1); lwait<1>(L, C_OK + 2, 1); lwait<1>(L, C_OK + 3, 1);
    STAMP(8);
    {
      const u32 fo = O_F2 + (dt * 28 + 7 * w + (lane < 7 ? lane : 6)) * 128;
      const v2u tg = *(LAS v2u*)(L + L_C + C_TAG * 4);
      _Pragma("unroll 1") for (u32 s = 0; s < SPIN_CAP; ++s) {
        asm volatile("" ::: "memory");
        v2u f;
        ld8s(f, fo, sw, 0);
        asm volatile("s_waitcnt vmcnt(0)" : "+v"(f)::"memory");
        if (__all(f.x == tg.x && f.y == tg.y)) break;
        __builtin_amdgcn_s_sleep(1);
      }
    }
    STAMP(9);
    auto uload = [&](int k) {
      u32 kg = k + rot2; kg = kg >= 28 ? kg - 28 : kg;
      const u32 so = O_U + (u32)(2 * dt * 112 + 28 * w + kg) * 1024u;
      ld16s(ring[2 * (k % RING)], lane * 16, sw, so);
      ld16s(ring[2 * (k % RING) + 1], lane * 16, sw, so + 112 * 1024);
    };
#pragma unroll
    for (int k = 0; k < RING; ++k) uload(k);
#pragma unroll
    for (int i = 0; i < 4; ++i)
#pragma unroll
      for (int j = 0; j < 16; ++j) acc[i][j] = 0.f;
    const int fs = 64 * st + ((tid >> 3) & 63);
    const u32 go = O_GA + fs * 1024 + dt * 128 + (tid & 7) * 16;
    {
      v4u wf[2][2];
      sfor<0, 28>([&](auto K) {
        constexpr int k = decltype(K)::v;
        waitv2<g2_after(k)>(ring[2 * (k % RING)], ring[2 * (k % RING) + 1]);
        if constexpr (k == 0) { STAMP(10); }
        constexpr int kn = k + 1;
        if constexpr (kn >= 12 && kn < 28) {
          constexpr u32 o = (kn < 16 ? kn : kn - 16) * 2048;
          wf[kn & 1][0] = trd2(L, t2 + o, t2 + o + 1024); wf[kn & 1][1] = trd2(L, (t2 ^ 64u) + o, (t2 ^ 64u) + o + 1024);
        }
        const v4u b0 = k < 12 ? wr_[(2 * k) % 24] : wf[k & 1][0];
        const v4u b1 = k < 12 ? wr_[(2 * k + 1) % 24] : wf[k & 1][1];
        acc[0] = mfma32(ring[2 * (k % RING)], b0, acc[0]);
        acc[1] = mfma32(ring[2 * (k % RING)], b1, acc[1]);
        acc[2] = mfma32(ring[2 * (k % RING) + 1], b0, acc[2]);
        acc[3] = mfma32(ring[2 * (k % RING) + 1], b1, acc[3]);
        SCHED();
        if constexpr (k + RING < 28) uload(k + RING);
        if constexpr (k == 22) {
          ld16s(fin_ga, go, sw, 0);
          asm volatile("buffer_load_dword %0, %1, %2, 0 offen sc1" : "=v"(fin_om) : "v"(O_OMG + fs * 4), "s"(sw) : "memory");
          const v4i sb = mksrd(p.b2, 2048 * 2);
          asm volatile("buffer_load_ushort %0, %1, %2, 0 offen" : "=v"(fin_b2) : "v"(fs * 2), "s"(sb) : "memory");
        }
        SCHED();
      });
    }
    STAMP(11);
#pragma unroll
    for (int md = 0; md < 2; ++md)
#pragma unroll
      for (int nt = 0; nt < 2; ++nt)
#pragma unroll
        for (int g = 0; g < 4; ++g) {
          const f16v& a = acc[2 * md + nt];
          const f4v v = {a[4 * g], a[4 * g + 1], a[4 * g + 2], a[4 * g + 3]};
          *(LAS f4v*)(L + lo + (32 * nt + (lane & 31)) * 272 + (32 * md + 8 * g + 4 * hh) * 4) = v;
        }
    asm volatile("s_waitcnt vmcnt(0)" : "+v"(fin_ga), "+v"(fin_om), "+v"(fin_b2)::"memory");
    _Pragma("unroll 1") for (u32 sp = 0; sp < SPIN_CAP && __any(max(max4(fin_ga), fin_om) == POISON); ++sp) {
      ld16s(fin_ga, go, sw, 0);
      asm volatile("buffer_load_dword %0, %1, %2, 0 offen sc1" : "=v"(fin_om) : "v"(O_OMG + fs * 4), "s"(sw) : "memory");
      asm volatile("s_waitcnt vmcnt(0)" : "+v"(fin_ga), "+v"(fin_om)::"memory");
    }
    STAMP(12);
  }
  WAITL();
  __builtin_amdgcn_s_barrier();
  asm volatile("" ::: "memory");
  {
    const int sl = tid >> 3;
    const u32 ro = L_G + sl * 272 + (tid & 7) * 32;
    f4v s0v = *(LAS f4v*)(L + ro), s1v = *(LAS f4v*)(L + ro + 16);
#pragma unroll
    for (int l = 1; l < 4; ++l) { s0v += *(LAS f4v*)(L + ro + l * 32768); s1v += *(LAS f4v*)(L + ro + l * 32768 + 16); }
    const float b2 = blo(fin_b2), om = __uint_as_float(fin_om);
    v4u o;
#pragma unroll
    for (int i = 0; i < 4; ++i) {
      const float e0 = i < 2 ? s0v[2 * i] : s1v[2 * i - 4], e1 = i < 2 ? s0v[2 * i + 1] : s1v[2 * i - 3];
      const u32 y = pk(e0, e1);
      const u32 yb = pk(blo(y) + b2, bhi(y) + b2);
      const u32 m = pk(om * blo(yb), om * bhi(yb));
      o[i] = pk(blo(fin_ga[i]) + blo(m), bhi(fin_ga[i]) + bhi(m));
    }
    *(v4u*)(p.out + (size_t)(64 * st + sl) * 512 + dt * 64 + (tid & 7) * 8) = o;
  }
}

extern "C" void kernel_launch(void* const* d_in, const int* in_sizes, int n_in, void* d_out, int out_size, void* d_ws, size_t ws_size,
                              hipStream_t stream) {
  Params p;
  p.x = (const u16*)d_in[0]; p.wr = (const u16*)d_in[1]; p.wi = (const u16*)d_in[2];
  p.w1 = (const u16*)d_in[4]; p.b1 = (const u16*)d_in[5]; p.w2 = (const u16*)d_in[6]; p.b2 = (const u16*)d_in[7]; p.gw = (const u16*)d_in[8];
  p.out = (u16*)d_out;
  p.ws = (char*)d_ws + ((ws_size - WS_BYTES) & ~(size_t)0xFFFFF);
  (void)hipFuncSetAttribute((const void*)fused_kernel, hipFuncAttributeMaxDynamicSharedMemorySize, LDS_BYTES);
  fused_kernel<<<256, 512, LDS_BYTES, stream>>>(p);
}
```

```cpp
#define DIAG
#define AMP_MODE 0
#include <hip/hip_runtime.h>
#include <stdint.h>

typedef unsigned u32;
typedef unsigned long long u64;
typedef unsigned short u16;
typedef u32 v4u __attribute__((ext_vector_type(4)));
typedef u32 v2u __attribute__((ext_vector_type(2)));
typedef int v4i __attribute__((ext_vector_type(4)));
typedef short s4v __attribute__((ext_vector_type(4)));
typedef short s8v __attribute__((ext_vector_type(8)));
typedef float f2v __attribute__((ext_vector_type(2)));
typedef float f4v __attribute__((ext_vector_type(4)));
typedef float f16v __attribute__((ext_vector_type(16)));
typedef __bf16 b2v __attribute__((ext_vector_type(2)));
#define LAS __attribute__((address_space(3)))
#define DI __device__ __forceinline__
typedef __amdgpu_buffer_rsrc_t rsrc_t;

constexpr u32 O_EPOCH = 0, O_F1 = 4096, O_F2 = 32768, O_XN = 65536, O_VN = 73728, O_OMG = 81920, O_DIAG = 131072;
constexpr u32 O_H = 1u << 20, O_X1 = 3u << 20, O_V = 5u << 20, O_GA = 7u << 20, O_U = 9u << 20, WS_BYTES = 11u << 20;
constexpr u32 POISON = 0xFFFFFFFFu;
constexpr int AUX_SC1 = 16;
constexpr u32 SPIN_CAP = 200000;
constexpr u32 L_G = 0;
constexpr u32 L_S = 131072;
constexpr u32 L_X = L_S, L_HR = L_S + 8704, L_T = L_S + 17408;
constexpr u32 L_E = L_S, L_W = L_S + 8704, L_O = L_S + 17408;
constexpr u32 L_U = L_S;
constexpr u32 L_C = L_S + 26624;
constexpr u32 LDS_BYTES = 163840;
enum { C_H1 = 0, C_F1 = 1, C_OK = 2  , C_P2A = 6, C_P2B = 7, C_P3A = 8, C_P3B = 9, C_R1 = 10, C_R2 = 11, C_F2 = 12, C_TAG = 14  , C_G = 16  , C_NY = 24  , C_N = 32 };

struct Params {
  const u16 *x, *wr, *wi, *w1, *b1, *w2, *b2, *gw;
  u16* out;
  char* ws;
};

DI u32 pk(float a, float b) { f2v v = {a, b}; return __builtin_bit_cast(u32, __builtin_convertvector(v, b2v)); }
DI float blo(u32 v) { return __uint_as_float(v << 16); }
DI float bhi(u32 v) { return __uint_as_float(v & 0xFFFF0000u); }
DI float rbf(float a) { return blo(pk(a, a)); }
DI float cosr(int n, float inv) { return __builtin_amdgcn_cosf((float)n * inv); }
DI float sinr(int n, float inv) { return __builtin_amdgcn_sinf((float)n * inv); }
DI u32 san(u32 v) { return v < 0xFFFFFFFEu ? v : 0xFFFFFFFEu; }
DI v4u san4(v4u v) { v4u r = {san(v.x), san(v.y), san(v.z), san(v.w)}; return r; }
DI u32 max4(v4u v) { return max(max(v.x, v.y), max(v.z, v.w)); }
DI f4v mfma16(v4u a, v4u b, f4v c) { return __builtin_amdgcn_mfma_f32_16x16x32_bf16(__builtin_bit_cast(s8v, a), __builtin_bit_cast(s8v, b), c, 0, 0, 0); }
DI f16v mfma32(v4u a, v4u b, f16v c) { return __builtin_amdgcn_mfma_f32_32x32x16_bf16(__builtin_bit_cast(s8v, a), __builtin_bit_cast(s8v, b), c, 0, 0, 0); }
DI v2u trd(LAS char* L, u32 off) { return __builtin_bit_cast(v2u, __builtin_amdgcn_ds_read_tr16_b64_v4i16((LAS s4v*)(L + off))); }
DI v4u trd2(LAS char* L, u32 o0, u32 o1) { v2u a = trd(L, o0), b = trd(L, o1); v4u r = {a.x, a.y, b.x, b.y}; return r; }
template <int C> DI float dppadd(float v) { return v + __int_as_float(__builtin_amdgcn_update_dpp(0, __float_as_int(v), C, 0xF, 0xF, true)); }
DI float wsum(float v) {
  v = dppadd<0xB1>(v); v = dppadd<0x4E>(v); v = dppadd<0x141>(v); v = dppadd<0x140>(v);
  auto a = __builtin_amdgcn_permlane16_swap(__float_as_uint(v), __float_as_uint(v), false, false);
  v = __uint_as_float(a[0]) + __uint_as_float(a[1]);
  auto b = __builtin_amdgcn_permlane32_swap(__float_as_uint(v), __float_as_uint(v), false, false);
  return __uint_as_float(b[0]) + __uint_as_float(b[1]);
}
DI u64 mix64(u64 z) { z += 0x9E3779B97F4A7C15ull; z = (z ^ (z >> 30)) * 0xBF58476D1CE4E5B9ull; z = (z ^ (z >> 27)) * 0x94D049BB133111EBull; return z ^ (z >> 31); }
#define WAITL() asm volatile("s_waitcnt lgkmcnt(0)" ::: "memory")
template <int N> DI void waitv() { asm volatile("s_waitcnt vmcnt(%0)" ::"n"(N) : "memory"); }
#define SCHED() __builtin_amdgcn_sched_barrier(0)
DI u32 larrive(LAS char* L, int c, int lane) {
  WAITL();
  u32 old = 0;
  if (lane == 0) old = __hip_atomic_fetch_add((LAS u32*)(L + L_C) + c, 1u, __ATOMIC_RELAXED, __HIP_MEMORY_SCOPE_WORKGROUP);
  return __builtin_amdgcn_readfirstlane(old);
}
template <int SL = 0> DI void lwait(LAS char* L, int c, u32 n) {
  volatile LAS u32* p = (volatile LAS u32*)(L + L_C) + c;
  _Pragma("unroll 1") for (u32 s = 0; (u32)__builtin_amdgcn_readfirstlane(*p) < n && s < 40u * SPIN_CAP; ++s) { if (SL) __builtin_amdgcn_s_sleep(SL); }
  asm volatile("" ::: "memory");
}
DI void lsync(LAS char* L, int c, int lane) { larrive(L, c, lane); lwait(L, c, 4); }

#define STAMP(i) ts[i] = __builtin_amdgcn_s_memrealtime()

template <int SGN> DI void dft8(const float (&ar)[8], const float (&ai)[8], float (&yr)[8], float (&yi)[8]) {
  const float R = 0.70710678118654752f, s = (float)SGN;
  float br[8], bi[8];
#pragma unroll
  for (int i = 0; i < 4; ++i) { br[i] = ar[i] + ar[i + 4]; bi[i] = ai[i] + ai[i + 4]; }
  br[4] = ar[0] - ar[4]; bi[4] = ai[0] - ai[4];
  { float r = ar[1] - ar[5], m = ai[1] - ai[5]; br[5] = R * (r - s * m); bi[5] = R * (m + s * r); }
  { float r = ar[2] - ar[6], m = ai[2] - ai[6]; br[6] = -s * m; bi[6] = s * r; }
  { float r = ar[3] - ar[7], m = ai[3] - ai[7]; br[7] = -R * (r + s * m); bi[7] = R * (s * r - m); }
#pragma unroll
  for (int h = 0; h < 2; ++h) {
    const int o = 4 * h;
    float er = br[o] + br[o + 2], ei = bi[o] + bi[o + 2], fr = br[o] - br[o + 2], fi = bi[o] - bi[o + 2];
    float gr = br[o + 1] + br[o + 3], gi = bi[o + 1] + bi[o + 3], hr = br[o + 1] - br[o + 3], hi = bi[o + 1] - bi[o + 3];
    yr[h] = er + gr; yi[h] = ei + gi; yr[h + 4] = er - gr; yi[h + 4] = ei - gi;
    yr[h + 2] = fr - s * hi; yi[h + 2] = fi + s * hr; yr[h + 6] = fr + s * hi; yi[h + 6] = fi - s * hr;
  }
}

struct P2C { v4u f1r, f1i, fr, fi, gr, gi, g1r, g1i; float tc[4], ts[4], uc[4], us[4]; };
DI v4u p2_column(const P2C& c, v4u in, v4u wv) {
  const f4v z = {0.f, 0.f, 0.f, 0.f};
  f4v r = mfma16(in, c.f1r, z), m = mfma16(in, c.f1i, z);
  v4u t;
#pragma unroll
  for (int i = 0; i < 4; ++i) t[i] = pk(r[i] * c.tc[i] + m[i] * c.ts[i], m[i] * c.tc[i] - r[i] * c.ts[i]);
  r = mfma16(c.fr, t, z); m = mfma16(c.fi, t, z);
#pragma unroll
  for (int i = 0; i < 4; ++i) { const float a = blo(wv[i]), b = bhi(wv[i]); t[i] = pk(r[i] * a - m[i] * b, r[i] * b + m[i] * a); }
  r = mfma16(t, c.gr, z); m = mfma16(t, c.gi, z);
#pragma unroll
  for (int i = 0; i < 4; ++i) t[i] = pk(r[i] * c.uc[i] - m[i] * c.us[i], m[i] * c.uc[i] + r[i] * c.us[i]);
  r = mfma16(c.g1r, t, z); m = mfma16(c.g1i, t, z);
#pragma unroll
  for (int i = 0; i < 4; ++i) t[i] = san(pk(r[i], m[i]));
  return t;
}

DI void dma16(u32 ldsaddr, u32 voff, v4i srd, u32 soff) {
  asm volatile("s_mov_b32 m0, %0\n\ts_nop 0\n\tbuffer_load_dwordx4 %1, %2, %3 offen lds" ::"s"(ldsaddr), "v"(voff), "s"(srd), "s"(soff) : "memory");
}
DI void ld16s(v4u& d, u32 voff, v4i srd, u32 soff) { asm volatile("buffer_load_dwordx4 %0, %1, %2, %3 offen sc1" : "=v"(d) : "v"(voff), "s"(srd), "s"(soff) : "memory"); }
DI void ld8s(v2u& d, u32 voff, v4i srd, u32 soff) { asm volatile("buffer_load_dwordx2 %0, %1, %2, %3 offen sc1" : "=v"(d) : "v"(voff), "s"(srd), "s"(soff) : "memory"); }
DI void ld8(v2u& d, u32 voff, v4i srd, u32 soff) { asm volatile("buffer_load_dwordx2 %0, %1, %2, %3 offen" : "=v"(d) : "v"(voff), "s"(srd), "s"(soff) : "memory"); }
DI void st16s(v4u d, u32 voff, v4i srd, u32 soff) { asm volatile("buffer_store_dwordx4 %0, %1, %2, %3 offen sc1\n\ts_nop 1" ::"v"(d), "v"(voff), "s"(srd), "s"(soff) : "memory"); }
DI void st8s(v2u d, u32 voff, v4i srd, u32 soff) { asm volatile("buffer_store_dwordx2 %0, %1, %2, %3 offen sc1\n\ts_nop 1" ::"v"(d), "v"(voff), "s"(srd), "s"(soff) : "memory"); }
template <int N> DI void waitv2(v4u& a, v4u& b) { asm volatile("s_waitcnt vmcnt(%2)" : "+v"(a), "+v"(b) : "n"(N) : "memory"); }
DI v4i mksrd(const void* p, u32 bytes) { const u64 a = (u64)p; v4i r = {(int)(u32)a, (int)((u32)(a >> 32) & 0xFFFFu), (int)bytes, 0x00020000}; return r; }

constexpr int RING = 6;
constexpr int g1_extra(int k) { return (k == 3 ? 8 : 0) + (k == 25 ? 4 : 0) + (k >= 26 && k <= 29 ? 4 : 0) + (k == 30 ? 4 : 0); }
constexpr int g1_after(int k) {
  int n = 0;
  if (k < RING) n += 2 * (RING - 1 - k);
  else n += g1_extra(k - RING);
  for (int j = (k < RING ? 0 : k - RING + 1); j < k; ++j) n += (j + RING < 32 ? 2 : 0) + g1_extra(j);
  return n;
}
constexpr int g2_extra(int k) { return k == 22 ? 3 : 0; }
constexpr int g2_after(int k) {
  int n = 0;
  if (k < RING) n += 2 * (RING - 1 - k);
  else n += g2_extra(k - RING);
  for (int j = (k < RING ? 0 : k - RING + 1); j < k; ++j) n += (j + RING < 28 ? 2 : 0) + g2_extra(j);
  return n;
}

template <int K> struct IC { static constexpr int v = K; };
template <int B, int E, class Fn> DI void sfor(Fn&& f) { if constexpr (B < E) { f(IC<B>{}); sfor<B + 1, E>(f); } }

__global__ void __launch_bounds__(512, 2) fused_kernel(Params p) {
  extern __shared__ __attribute__((aligned(16))) char smem[];
  LAS char* L = (LAS char*)smem;
  const int tid = threadIdx.x, lane = tid & 63, wv = __builtin_amdgcn_readfirstlane(tid >> 6), w = wv & 3;
  const int b = blockIdx.x, xl = b & 7, idx = b >> 3;
  const int dtl = idx & 3, dt = 4 * (xl >> 2) + dtl, grp = xl & 3;
  const int st = 8 * grp + (idx >> 2);
  u64 ts[16];
#pragma unroll
  for (int i = 0; i < 16; ++i) ts[i] = 0;
  STAMP(0);
  const u64 clk0 = __builtin_amdgcn_s_memtime();
  const rsrc_t rw = __builtin_amdgcn_make_buffer_rsrc((void*)p.ws, 0, (int)WS_BYTES, 0x00020000);
  asm volatile("" ::"s"(p.x), "s"(p.wr), "s"(p.wi), "s"(p.w1), "s"(p.b1), "s"(p.w2), "s"(p.b2), "s"(p.gw), "s"(p.out), "s"(p.ws));
  v4u fin_ga; u32 fin_om, fin_b2;

  if (wv < 4) {
    __builtin_amdgcn_s_setprio(3);
    const int s0 = b, q = lane >> 4, c = lane & 15;
    const rsrc_t rx = __builtin_amdgcn_make_buffer_rsrc((void*)p.x, 0, 2048 * 512 * 2, 0x00020000);
    const v4u x0 = __builtin_amdgcn_raw_buffer_load_b128(rx, lane * 16, (s0 + 512 * w) * 1024, 0);
    const v4u x1 = __builtin_amdgcn_raw_buffer_load_b128(rx, lane * 16, (s0 + 512 * w + 256) * 1024, 0);
    const v4u gwv = *(const v4u*)(p.gw + lane * 8);
    const v2u epv = __builtin_amdgcn_raw_buffer_load_b64(rw, O_EPOCH, 0, 0);
    {
      const v4u P = {POISON, POISON, POISON, POISON};
      __builtin_amdgcn_raw_buffer_store_b128(P, rw, lane * 16, O_X1 + (2 * w) * 262144 + s0 * 1024, AUX_SC1);
      __builtin_amdgcn_raw_buffer_store_b128(P, rw, lane * 16, O_X1 + (2 * w + 1) * 262144 + s0 * 1024, AUX_SC1);
      __builtin_amdgcn_raw_buffer_store_b128(P, rw, lane * 16, O_V + b * 8192 + w * 2048, AUX_SC1);
      __builtin_amdgcn_raw_buffer_store_b128(P, rw, lane * 16, O_V + b * 8192 + w * 2048 + 1024, AUX_SC1);
      __builtin_amdgcn_raw_buffer_store_b128(P, rw, lane * 16, O_GA + b * 8192 + w * 2048, AUX_SC1);
      __builtin_amdgcn_raw_buffer_store_b128(P, rw, lane * 16, O_GA + b * 8192 + w * 2048 + 1024, AUX_SC1);
      if (w == 0 && lane < 24)
        __builtin_amdgcn_raw_buffer_store_b32(POISON, rw, lane < 8 ? O_XN + lane * 1024 + s0 * 4 : (lane < 16 ? O_VN : O_OMG) + b * 32 + (lane & 7) * 4, 0, AUX_SC1);
    }
    if (tid < C_N) ((LAS u32*)(L + L_C))[tid] = 0;
    WAITL();
    __builtin_amdgcn_s_barrier();
    float xa[8], xb[8];
#pragma unroll
    for (int i = 0; i < 4; ++i) { xa[2 * i] = blo(x0[i]); xa[2 * i + 1] = bhi(x0[i]); xb[2 * i] = blo(x1[i]); xb[2 * i + 1] = bhi(x1[i]); }
    STAMP(1);
    *(LAS v4u*)(L + L_X + (2 * w) * 1088 + lane * 16) = x0;
    *(LAS v4u*)(L + L_X + (2 * w + 1) * 1088 + lane * 16) = x1;
    {
      float sa = ((xa[0] + xa[1]) + (xa[2] + xa[3])) + ((xa[4] + xa[5]) + (xa[6] + xa[7]));
      float sb = ((xb[0] + xb[1]) + (xb[2] + xb[3])) + ((xb[4] + xb[5]) + (xb[6] + xb[7]));
      const float ma = wsum(sa) * (1.f / 512.f), mb = wsum(sb) * (1.f / 512.f);
      float va = 0.f, vb = 0.f;
#pragma unroll
      for (int i = 0; i < 8; ++i) { const float da = xa[i] - ma, db = xb[i] - mb; va += da * da; vb += db * db; }
      va = wsum(va) * (1.f / 512.f); vb = wsum(vb) * (1.f / 512.f);
      const float ra = __builtin_amdgcn_rcpf(rbf(__builtin_amdgcn_sqrtf(rbf(rbf(va) + 1e-5f))));
      const float rb = __builtin_amdgcn_rcpf(rbf(__builtin_amdgcn_sqrtf(rbf(rbf(vb) + 1e-5f))));
      const float na = -rbf(ma) * ra, nb = -rbf(mb) * rb;
      v4u ha, hb;
#pragma unroll
      for (int i = 0; i < 4; ++i) {
        ha[i] = pk(xa[2 * i] * ra + na, xa[2 * i + 1] * ra + na);
        hb[i] = pk(xb[2 * i] * rb + nb, xb[2 * i + 1] * rb + nb);
      }
      *(LAS v4u*)(L + L_HR + (2 * w) * 1088 + lane * 16) = ha;
      *(LAS v4u*)(L + L_HR + (2 * w + 1) * 1088 + lane * 16) = hb;
    }
    lsync(L, C_H1, lane);
    STAMP(11);
    {
      const u32 ta = L_HR + (c >> 2) * 1088 + (128 * w + 16 * q + 4 * (c & 3)) * 2;
      const v4u h0 = trd2(L, ta, ta + 4 * 1088), h1 = trd2(L, ta + 128, ta + 128 + 4 * 1088);
      const u32 vo = (lane >> 5) * 131072 + (lane & 31) * 16;
      __builtin_amdgcn_raw_buffer_store_b128(h0, rw, vo, O_H + (4 * w) * 131072 + s0 * 512, AUX_SC1);
      __builtin_amdgcn_raw_buffer_store_b128(h1, rw, vo, O_H + (4 * w + 2) * 131072 + s0 * 512, AUX_SC1);
    }
    STAMP(2);
    float om_keep;
    {
      float ga = 0.f, gb = 0.f;
#pragma unroll
      for (int i = 0; i < 4; ++i) {
        const float g0 = blo(gwv[i]), g1 = bhi(gwv[i]);
        const u32 pa = pk(xa[2 * i] * g0, xa[2 * i + 1] * g1), pb = pk(xb[2 * i] * g0, xb[2 * i + 1] * g1);
        ga += blo(pa) + bhi(pa); gb += blo(pb) + bhi(pb);
      }
      ga = rbf(wsum(ga)); gb = rbf(wsum(gb));
      const float gs = (lane & 1) ? gb : ga;
      const float g = rbf(__builtin_amdgcn_rcpf(1.f + __builtin_amdgcn_exp2f(-1.44269504088896f * gs)));
      om_keep = rbf(1.f - g);
      if (lane < 2) ((LAS float*)(L + L_C))[C_G + 2 * w + lane] = g;
    }
    v4u f64r[2], f64i[2], a8;
    float twc[4], tws[4];
    {
      const int k1 = 16 * w + c;
#pragma unroll
      for (int ks = 0; ks < 2; ++ks)
#pragma unroll
        for (int j = 0; j < 4; ++j) {
          const int a = 32 * ks + 8 * q + 2 * j, n0 = (a * k1) & 63, n1 = ((a + 1) * k1) & 63;
          f64r[ks][j] = pk(cosr(n0, 1.f / 64), cosr(n1, 1.f / 64));
          f64i[ks][j] = pk(-sinr(n0, 1.f / 64), -sinr(n1, 1.f / 64));
        }
#pragma unroll
      for (int r = 0; r < 4; ++r) {
        const int bb = 4 * (q & 1) + r, n = (bb * (c >> 2)) & 7;
        const float cs = cosr(n, 0.125f), sn = sinr(n, 0.125f);
        const u32 e = (c & 1) ? pk(-sn, cs) : pk(cs, sn);
        a8[r] = (((c >> 1) & 1) == (q >> 1)) ? e : 0u;
        twc[r] = cosr(bb * k1, 1.f / 512); tws[r] = sinr(bb * k1, 1.f / 512);
      }
    }
    const u64 tag = mix64(((u64)(u32)__builtin_amdgcn_readfirstlane(epv.y) << 32) | (u32)__builtin_amdgcn_readfirstlane(epv.x));
    if (lane == 0) { const v2u tg = {(u32)tag, (u32)(tag >> 32)}; *(LAS v2u*)(L + L_C + C_TAG * 4) = tg; }
    STAMP(12);
    waitv<0>();
    STAMP(3);
    if (larrive(L, C_F1, lane) == 3 && lane < 8) {
      const v2u tg = {(u32)tag, (u32)(tag >> 32)};
      __builtin_amdgcn_raw_buffer_store_b64(tg, rw, O_F1 + lane * 2048 + b * 8, 0, AUX_SC1);
    }
    const int j1 = xl, kb = idx;
    v4u wq0, wq1;
    {
      const rsrc_t rr = __builtin_amdgcn_make_buffer_rsrc((void*)p.wr, 0, 2048 * 257 * 2, 0x00020000);
      const rsrc_t ri = __builtin_amdgcn_make_buffer_rsrc((void*)p.wi, 0, 2048 * 257 * 2, 0x00020000);
      const u32 o = (u32)(j1 + 8 * (64 * w + lane)) * 514u + kb * 16;
      wq0 = __builtin_amdgcn_raw_buffer_load_b128(rr, o, 0, 0);
      wq1 = __builtin_amdgcn_raw_buffer_load_b128(ri, o, 0, 0);
    }
    {
      float yr[8], yi[8], nr[8], ni[8];
      const f4v z = {0.f, 0.f, 0.f, 0.f};
      const u32 xo = L_X + ((c >> 1) & 1) * 1088 + (8 * q + (c >> 2)) * 16 + 8 * (c & 1);
      v4u a8n = a8; a8n[1] ^= 0x80008000u; a8n[3] ^= 0x80008000u;
#pragma unroll
      for (int pi = 0; pi < 4; ++pi) {
        const v4u A0 = trd2(L, xo + pi * 2176, xo + pi * 2176 + 64), A1 = trd2(L, xo + pi * 2176 + 512, xo + pi * 2176 + 576);
        f4v r = mfma16(A0, f64r[0], z), m = mfma16(A0, f64i[0], z);
        r = mfma16(A1, f64r[1], r); m = mfma16(A1, f64i[1], m);
        v4u t;
#pragma unroll
        for (int i = 0; i < 4; ++i) t[i] = pk(r[i] * twc[i] + m[i] * tws[i], m[i] * twc[i] - r[i] * tws[i]);
        const f4v d = mfma16(a8, t, z);
        yr[2 * pi] = d[0]; yi[2 * pi] = d[1]; yr[2 * pi + 1] = d[2]; yi[2 * pi + 1] = d[3];
        if (w == 0) { const f4v e = mfma16(a8n, t, z); nr[2 * pi] = e[0]; ni[2 * pi] = e[1]; nr[2 * pi + 1] = e[2]; ni[2 * pi + 1] = e[3]; }
      }
      float zr[8], zi[8];
      dft8<-1>(yr, yi, zr, zi);
#pragma unroll
      for (int j = 0; j < 8; ++j) *(LAS u32*)(L + L_T + w * 2048 + j * 256 + lane * 4) = san(pk(zr[j], zi[j]));
      const v4u o0 = *(LAS v4u*)(L + L_T + w * 2048 + lane * 16), o1 = *(LAS v4u*)(L + L_T + w * 2048 + 1024 + lane * 16);
      STAMP(13);
      lwait(L, C_F1, 4);
      const u32 vo = q * 262144 + (((lane >> 2) & 3) * 64 + (lane & 3) * 4) * 4;
      __builtin_amdgcn_raw_buffer_store_b128(o0, rw, vo, O_X1 + s0 * 1024 + w * 64, AUX_SC1);
      __builtin_amdgcn_raw_buffer_store_b128(o1, rw, vo, O_X1 + 4 * 262144 + s0 * 1024 + w * 64, AUX_SC1);
      if (w == 0) {
        dft8<-1>(nr, ni, zr, zi);
        if (lane == 0) {
#pragma unroll
          for (int j = 0; j < 8; ++j) __builtin_amdgcn_raw_buffer_store_b32(san(pk(zr[j], zi[j])), rw, O_XN + j * 1024 + s0 * 4, 0, AUX_SC1);
        }
      }
    }
    STAMP(4);
    const u32 epos = ((w * 16 + c) * 4 + q) * 4;
    P2C pc;
    {
#pragma unroll
      for (int i = 0; i < 4; ++i) {
        const int e = 4 * q + i;
        { const int n = (e * (8 * c + j1)) & 127; const float cs = cosr(n, 1.f / 128), sn = sinr(n, 1.f / 128); pc.f1r[i] = pk(cs, sn); pc.f1i[i] = pk(-sn, cs); }
        { const int n = (e * c) & 15; const float cs = cosr(n, 1.f / 16), sn = sinr(n, 1.f / 16); pc.fr[i] = pk(cs, sn); pc.fi[i] = pk(-sn, cs); pc.gr[i] = pk(cs, -sn); pc.gi[i] = pk(sn, cs); }
        { const int n = (c * (8 * e + j1)) & 127; const float cs = cosr(n, 1.f / 128), sn = sinr(n, 1.f / 128); pc.g1r[i] = pk(cs, -sn); pc.g1i[i] = pk(sn, cs); }
        pc.tc[i] = cosr(e * (8 * c + j1), 1.f / 2048); pc.ts[i] = sinr(e * (8 * c + j1), 1.f / 2048);
        pc.uc[i] = cosr(c * (8 * e + j1), 1.f / 2048); pc.us[i] = sinr(c * (8 * e + j1), 1.f / 2048);
      }
#pragma unroll
      for (int i = 0; i < 4; ++i) {
        u32 lo = __builtin_amdgcn_perm(wq1[i], wq0[i], 0x05040100u), hi = __builtin_amdgcn_perm(wq1[i], wq0[i], 0x07060302u);
        if (i == 0 && kb == 0) lo = pk(0.5f * blo(lo), 0.5f * bhi(lo));
        *(LAS u32*)(L + L_W + (2 * i) * 1024 + epos) = lo;
        *(LAS u32*)(L + L_W + (2 * i + 1) * 1024 + epos) = hi;
      }
    }
    {
      const u32 fo = O_F1 + xl * 2048 + (64 * w + lane) * 8;
      _Pragma("unroll 1") for (u32 s = 0; s < SPIN_CAP; ++s) {
        asm volatile("" ::: "memory");
        const v2u f = __builtin_amdgcn_raw_buffer_load_b64(rw, fo, 0, AUX_SC1);
        if (__all(f.x == (u32)tag && f.y == (u32)(tag >> 32))) break;
        __builtin_amdgcn_s_sleep(2);
      }
      asm volatile("" ::: "memory");
      if (lane == 0) { ((volatile LAS u32*)(L + L_C))[C_OK + w] = 1u; }
      STAMP(5);
      lwait(L, C_OK + 0, 1); lwait(L, C_OK + 1, 1); lwait(L, C_OK + 2, 1); lwait(L, C_OK + 3, 1);
    }
    if (b == 0 && tid == 0) *(u64*)(p.ws + O_EPOCH) = tag;
    if (lane < 2) __builtin_amdgcn_raw_buffer_store_b32(__float_as_uint(om_keep), rw, O_OMG + (s0 + 256 * (2 * w + lane)) * 4, 0, AUX_SC1);
    {
      const u32 xo = O_X1 + j1 * 262144 + (64 * w + lane) * 1024 + kb * 32;
      v4u e0 = __builtin_amdgcn_raw_buffer_load_b128(rw, xo, 0, AUX_SC1), e1 = __builtin_amdgcn_raw_buffer_load_b128(rw, xo + 16, 0, AUX_SC1);
      _Pragma("unroll 1") for (u32 s = 0; s < SPIN_CAP; ++s) {
        asm volatile("" ::: "memory");
        const bool bad = max(max4(e0), max4(e1)) == POISON;
        if (!__any(bad)) break;
        __builtin_amdgcn_s_sleep(1);
        if (bad) { e0 = __builtin_amdgcn_raw_buffer_load_b128(rw, xo, 0, AUX_SC1); e1 = __builtin_amdgcn_raw_buffer_load_b128(rw, xo + 16, 0, AUX_SC1); }
      }
      STAMP(6);
#pragma unroll
      for (int i = 0; i < 4; ++i) { *(LAS u32*)(L + L_E + i * 1024 + epos) = e0[i]; *(LAS u32*)(L + L_E + (4 + i) * 1024 + epos) = e1[i]; }
      lsync(L, C_P2A, lane);
      const v4u i0 = *(LAS v4u*)(L + L_E + (2 * w) * 1024 + lane * 16), i1 = *(LAS v4u*)(L + L_E + (2 * w + 1) * 1024 + lane * 16);
      const v4u w0 = *(LAS v4u*)(L + L_W + (2 * w) * 1024 + lane * 16), w1 = *(LAS v4u*)(L + L_W + (2 * w + 1) * 1024 + lane * 16);
      *(LAS v4u*)(L + L_O + (2 * w) * 1024 + lane * 16) = p2_column(pc, i0, w0);
      *(LAS v4u*)(L + L_O + (2 * w + 1) * 1024 + lane * 16) = p2_column(pc, i1, w1);
      lsync(L, C_P2B, lane);
      v4u o0, o1;
#pragma unroll
      for (int i = 0; i < 4; ++i) { o0[i] = *(LAS u32*)(L + L_O + i * 1024 + epos); o1[i] = *(LAS u32*)(L + L_O + (4 + i) * 1024 + epos); }
      STAMP(14);
      const u32 vo = O_V + (64 * w + lane) * 8192 + j1 * 1024 + kb * 32;
      __builtin_amdgcn_raw_buffer_store_b128(o0, rw, vo, 0, AUX_SC1);
      __builtin_amdgcn_raw_buffer_store_b128(o1, rw, vo + 16, 0, AUX_SC1);
      if (kb == 31 && w == 0) {
        v4u in, wn;
#pragma unroll
        for (int i = 0; i < 4; ++i) {
          const u32 e = 64 * q + 16 * i + c, o = (j1 + 8 * e) * 257 + 256;
          wn[i] = pk(0.5f * blo(p.wr[o]), 0.5f * blo(p.wi[o]));
          in[i] = POISON;
        }
        _Pragma("unroll 1") for (u32 s = 0; s < SPIN_CAP; ++s) {
        asm volatile("" ::: "memory");
#pragma unroll
          for (int i = 0; i < 4; ++i) in[i] = __builtin_amdgcn_raw_buffer_load_b32(rw, O_XN + j1 * 1024 + (64 * q + 16 * i + c) * 4, 0, AUX_SC1);
          if (!__any(max4(in) == POISON)) break;
        }
        const v4u o = p2_column(pc, in, wn);
#pragma unroll
        for (int i = 0; i < 4; ++i) __builtin_amdgcn_raw_buffer_store_b32(o[i], rw, O_VN + (64 * q + 16 * i + c) * 32 + j1 * 4, 0, AUX_SC1);
      }
    }
    STAMP(7);
    {
      v4u gB[4];
      float pcs[2], psn[2];
      u32 ax;
      {
        const int ap = 16 * w + c;
#pragma unroll
        for (int ks = 0; ks < 4; ++ks)
#pragma unroll
          for (int i = 0; i < 4; ++i) {
            const int n = ((16 * ks + 4 * q + i) * ap) & 63;
            gB[ks][i] = pk(cosr(n, 1.f / 64) * 0x1p-19f, -sinr(n, 1.f / 64) * 0x1p-19f);
          }
        const int n = (q * (c >> 1)) & 7;
        const float cs = cosr(n, 0.125f), sn = sinr(n, 0.125f);
        ax = (c & 1) ? pk(sn, cs) : pk(cs, -sn);
#pragma unroll
        for (int e = 0; e < 2; ++e) { pcs[e] = cosr(ap * (2 * q + e), 1.f / 512); psn[e] = sinr(ap * (2 * q + e), 1.f / 512); }
      }
      const u32 vo = O_V + s0 * 8192 + (16 * w + 64 * q + c) * 4;
      u32 vv[8];
#pragma unroll
      for (int j = 0; j < 8; ++j) vv[j] = __builtin_amdgcn_raw_buffer_load_b32(rw, vo + j * 1024, 0, AUX_SC1);
      _Pragma("unroll 1") for (u32 s = 0; s < SPIN_CAP; ++s) {
        asm volatile("" ::: "memory");
        const bool bad = max(max(max(vv[0], vv[1]), max(vv[2], vv[3])), max(max(vv[4], vv[5]), max(vv[6], vv[7]))) == POISON;
        if (!__any(bad)) break;
        __builtin_amdgcn_s_sleep(1);
        if (bad) {
#pragma unroll
          for (int j = 0; j < 8; ++j) vv[j] = __builtin_amdgcn_raw_buffer_load_b32(rw, vo + j * 1024, 0, AUX_SC1);
        }
      }
      STAMP(8);
      float ar[8], ai[8], yr[8], yi[8];
#pragma unroll
      for (int j = 0; j < 8; ++j) { ar[j] = blo(vv[j]); ai[j] = bhi(vv[j]); }
      dft8<1>(ar, ai, yr, yi);
      v4u yq[2];
#pragma unroll
      for (int i = 0; i < 8; ++i) yq[i >> 2][i & 3] = pk(yr[i], yi[i]);
      const f4v z = {0.f, 0.f, 0.f, 0.f};
#pragma unroll
      for (int i = 0; i < 8; ++i) {
        v4u A = {0u, 0u, 0u, 0u};
        A[i & 3] = ax;
        const f4v d = mfma16(A, yq[i >> 2], z);
        *(LAS u32*)(L + L_U + i * 2176 + (2 * q) * 272 + (16 * w + c) * 4) = pk(d[0] * pcs[0] - d[1] * psn[0], d[1] * pcs[0] + d[0] * psn[0]);
        *(LAS u32*)(L + L_U + i * 2176 + (2 * q + 1) * 272 + (16 * w + c) * 4) = pk(d[2] * pcs[1] - d[3] * psn[1], d[3] * pcs[1] + d[2] * psn[1]);
      }
      if (w == 0) {
        v4u n0, n1;
        _Pragma("unroll 1") for (u32 s = 0; s < SPIN_CAP; ++s) {
        asm volatile("" ::: "memory");
          n0 = __builtin_amdgcn_raw_buffer_load_b128(rw, O_VN + s0 * 32, 0, AUX_SC1);
          n1 = __builtin_amdgcn_raw_buffer_load_b128(rw, O_VN + s0 * 32 + 16, 0, AUX_SC1);
          if (!__any(max(max4(n0), max4(n1)) == POISON)) break;
        }
#pragma unroll
        for (int j = 0; j < 4; ++j) { ar[j] = blo(n0[j]); ai[j] = bhi(n0[j]); ar[j + 4] = blo(n1[j]); ai[j + 4] = bhi(n1[j]); }
        dft8<1>(ar, ai, yr, yi);
        if (lane == 0) {
#pragma unroll
          for (int i = 0; i < 8; ++i) ((LAS float*)(L + L_C))[C_NY + i] = yr[i] * 0x1p-19f;
        }
      }
      lsync(L, C_P3B, lane);
      const u32 uo = L_U + (c >> 3) * 2176 + (c & 7) * 272 + q * 16;
#pragma unroll
      for (int pi = 0; pi < 4; ++pi) {
        f4v d = z;
#pragma unroll
        for (int ks = 0; ks < 4; ++ks) d = mfma16(*(LAS v4u*)(L + uo + pi * 4352 + ks * 64), gB[ks], d);
        const int i = 2 * pi + (q >> 1);
        const float ny = ((LAS float*)(L + L_C))[C_NY + i], g = ((LAS float*)(L + L_C))[C_G + i];
        const u32 a01 = pk(d[0] + ny, d[1] - ny), a23 = pk(d[2] + ny, d[3] - ny);
        const v2u o = {san(pk(g * blo(a01), g * bhi(a01))), san(pk(g * blo(a23), g * bhi(a23)))};
        __builtin_amdgcn_raw_buffer_store_b64(o, rw, O_GA + (s0 + 256 * i) * 1024 + ((16 * w + c) * 8 + 4 * (q & 1)) * 2, 0, AUX_SC1);
      }
    }
    STAMP(9);
    {
      const int sl = (tid >> 3) & 63, s = 64 * st + sl;
      fin_b2 = p.b2[s];
      const u32 go = O_GA + s * 1024 + dt * 128 + (tid & 7) * 16;
      _Pragma("unroll 1") for (u32 sp = 0; sp < SPIN_CAP; ++sp) {
        asm volatile("" ::: "memory");
        fin_ga = __builtin_amdgcn_raw_buffer_load_b128(rw, go, 0, AUX_SC1);
        fin_om = __builtin_amdgcn_raw_buffer_load_b32(rw, O_OMG + s * 4, 0, AUX_SC1);
        if (!__any(max(max4(fin_ga), fin_om) == POISON)) break;
        __builtin_amdgcn_s_sleep(4);
      }
    }
    STAMP(10);
  } else {
    __builtin_amdgcn_s_barrier();
    const v4i sw = mksrd(p.ws, WS_BYTES), s2 = mksrd(p.w2, 1792u * 4096u);
    u32 lb = (u32)(size_t)smem + L_G + w * 32768, w2b = (u32)(448 * w) * 4096u + st * 128, rot2 = 7 * dtl;
    const u32 lo = L_G + w * 32768;
    const int hh = lane >> 5, blk = (lane >> 4) & 1, tq = (lane & 15) >> 2, tp = lane & 3;
    const u32 v2off = (lane >> 3) * 4096 + (((lane & 7) ^ (((lane >> 4) & 1) << 2)) * 16);
    auto w2dma = [&](int kl, u32 dst) {
      u32 kg = kl + rot2; kg = kg >= 28 ? kg - 28 : kg;
      const u32 so = w2b + kg * 65536u;
      dma16(lb + dst, v2off, s2, so);
      dma16(lb + dst + 1024, v2off, s2, so + 8 * 4096);
    };
    const u32 t2 = lo + (4 * hh + tq) * 128 + (((2 * blk + (tp >> 1)) ^ (((tq >> 1) & 1) << 2)) * 16) + 8 * (tp & 1);
    v4u wr_[24];
    f16v acc[4];
    v4u ring[2 * RING];
    if (idx < 28) {
      const int ft = 7 * grp + (idx >> 2);
      const v4i s1 = mksrd(p.w1, 2048u * 3584u);
      u32 v1off[4];
#pragma unroll
      for (int j = 0; j < 4; ++j) v1off[j] = ((lane >> 3) ^ (j >> 1)) * 3584 + ((lane & 7) ^ ((j & 1) << 2)) * 16;
      auto w1dma = [&](int bl, int slot) {
        const u32 so = (u32)(64 * w + 8 * ((bl + 2 * dtl) & 7)) * 3584u + ft * 128;
#pragma unroll
        for (int j = 0; j < 8; ++j) dma16(lb + slot * 8192 + j * 1024, v1off[j & 3], s1, so + j * (256u * 3584u));
      };
      const u32 t1 = lo + tq * 1024 + ((hh ^ (tq >> 1)) * 128) + ((((w >> 1) * 4 + 2 * blk + (tp >> 1)) ^ ((tq & 1) << 2)) * 16) + 8 * (tp & 1);
      const u32 t1x = t1 ^ 64u;
      w1dma(0, 0); w1dma(1, 1); STAMP(13); w1dma(2, 2); w1dma(3, 3);
      STAMP(1);
      sfor<0, 3>([&](auto B) {
        constexpr int bl = decltype(B)::v;
        waitv<24>();
#pragma unroll
        for (int k = 0; k < 4; ++k) {
          wr_[8 * bl + 2 * k] = trd2(L, t1 + bl * 8192 + k * 256, t1 + bl * 8192 + k * 256 + 4096);
          wr_[8 * bl + 2 * k + 1] = trd2(L, t1x + bl * 8192 + k * 256, t1x + bl * 8192 + k * 256 + 4096);
        }
        WAITL();
        w1dma(4 + bl, bl);
      });
      STAMP(2);
      lwait<1>(L, C_OK + w, 1);
      STAMP(3);
      auto hload = [&](int k) {
        const int pos = k >> 2, bl = pos < 4 ? pos + 3 : (pos < 7 ? pos - 4 : 7);
        const u32 so = O_H + (u32)(64 * w + 8 * ((bl + 2 * dtl) & 7) + 2 * (k & 3)) * 512u;
        ld16s(ring[2 * (k % RING)], lane * 16, sw, so + (2 * dt + (w & 1)) * 131072);
        ld16s(ring[2 * (k % RING) + 1], lane * 16, sw, so + (2 * dt + ((w & 1) ^ 1)) * 131072);
      };
#pragma unroll
      for (int k = 0; k < RING; ++k) hload(k);
#pragma unroll
      for (int i = 0; i < 4; ++i)
#pragma unroll
        for (int j = 0; j < 16; ++j) acc[i][j] = 0.f;
      v2u bias[4];
      v4u wf[2][2];
      sfor<0, 32>([&](auto K) {
        constexpr int k = decltype(K)::v, pos = k >> 2;
        waitv2<g1_after(k)>(ring[2 * (k % RING)], ring[2 * (k % RING) + 1]);
        if constexpr (k == 0) {
          STAMP(4);
          wf[0][0] = trd2(L, t1 + 3 * 8192, t1 + 3 * 8192 + 4096); wf[0][1] = trd2(L, t1x + 3 * 8192, t1x + 3 * 8192 + 4096);
        }
        constexpr int kn = k + 1, pn = kn >> 2;
        if constexpr (kn < 32 && (pn < 4 || pn == 7)) {
          constexpr u32 o = (pn == 0 || pn == 7 ? 3 : pn - 1) * 8192 + (kn & 3) * 256;
          wf[kn & 1][0] = trd2(L, t1 + o, t1 + o + 4096); wf[kn & 1][1] = trd2(L, t1x + o, t1x + o + 4096);
        }
        const v4u a0 = (pos >= 4 && pos < 7) ? wr_[2 * ((k - 16) & 15) % 24] : wf[k & 1][0];
        const v4u a1 = (pos >= 4 && pos < 7) ? wr_[(2 * ((k - 16) & 15) + 1) % 24] : wf[k & 1][1];
        acc[0] = mfma32(a0, ring[2 * (k % RING)], acc[0]);
        acc[1] = mfma32(a0, ring[2 * (k % RING) + 1], acc[1]);
        acc[2] = mfma32(a1, ring[2 * (k % RING)], acc[2]);
        acc[3] = mfma32(a1, ring[2 * (k % RING) + 1], acc[3]);
        SCHED();
        if constexpr (k + RING < 32) hload(k + RING);
        if constexpr (k == 15) { STAMP(14); }
        if constexpr (k == 3) { WAITL(); w1dma(7, 3); }
        if constexpr (k == 25) {
          const v4i sb = mksrd(p.b1, 1792 * 2);
#pragma unroll
          for (int g = 0; g < 4; ++g) ld8(bias[g], (8 * g + 4 * hh) * 2, sb, (64 * ft + 32 * (w >> 1)) * 2);
        }
        if constexpr (k >= 26 && k <= 30) { w2dma(2 * (k - 26), 2 * (k - 26) * 2048); w2dma(2 * (k - 26) + 1, (2 * (k - 26) + 1) * 2048); }
        SCHED();
      });
      STAMP(5);
#pragma unroll
      for (int l = 1; l < 4; ++l)
#pragma unroll
        for (int g = 0; g < 4; ++g) {
          const f4v v = {acc[l][4 * g], acc[l][4 * g + 1], acc[l][4 * g + 2], acc[l][4 * g + 3]};
          *(LAS f4v*)(L + lo + 20480 + (l - 1) * 4096 + g * 1024 + lane * 16) = v;
        }
      lsync(L, C_R1, lane);
#pragma unroll
      for (int l = 1; l < 4; ++l)
#pragma unroll
        for (int g = 0; g < 4; ++g) {
          const f4v v = *(LAS f4v*)(L + L_G + (w ^ l) * 32768 + 20480 + (l - 1) * 4096 + g * 1024 + lane * 16);
#pragma unroll
          for (int e = 0; e < 4; ++e) acc[0][4 * g + e] += v[e];
        }
      larrive(L, C_R2, lane);
      asm volatile("s_waitcnt vmcnt(20)" : "+v"(bias[0]), "+v"(bias[1]), "+v"(bias[2]), "+v"(bias[3])::"memory");
      v4u uf[2];
#pragma unroll
      for (int g = 0; g < 4; ++g) {
        float o[4];
#pragma unroll
        for (int e = 0; e < 4; ++e) {
          const float v = acc[0][4 * g + e] + ((e & 1) ? bhi(bias[g][e >> 1]) : blo(bias[g][e >> 1]));
          const float t = v * (-2.3022081985f - 0.1029432397f * v * v);
          o[e] = v * __builtin_amdgcn_rcpf(1.f + __builtin_amdgcn_exp2f(t));
        }
        uf[g >> 1][2 * (g & 1)] = pk(o[0], o[1]); uf[g >> 1][2 * (g & 1) + 1] = pk(o[2], o[3]);
      }
      const u32 uo = O_U + (u32)((2 * dt + (w & 1)) * 112 + 4 * ft + 2 * (w >> 1)) * 1024u;
      st16s(uf[0], lane * 16, sw, uo);
      st16s(uf[1], lane * 16, sw, uo + 1024);
      STAMP(6);
      waitv<2>();
#pragma unroll
      for (int k = 0; k < 10; ++k) { wr_[2 * k] = trd2(L, t2 + k * 2048, t2 + k * 2048 + 1024); wr_[2 * k + 1] = trd2(L, (t2 ^ 64u) + k * 2048, (t2 ^ 64u) + k * 2048 + 1024); }
      WAITL();
#pragma unroll
      for (int k = 16; k < 20; ++k) w2dma(k, (k - 16) * 2048);
      waitv<8>();
      if (larrive(L, C_F2, lane) == 3 && lane == 0) st8s(*(LAS v2u*)(L + L_C + C_TAG * 4), 0, sw, O_F2 + (dt * 28 + ft) * 128);
      STAMP(7);
      lwait(L, C_R2, 4);
#pragma unroll
      for (int k = 10; k < 16; ++k) w2dma(k, k * 2048);
#pragma unroll
      for (int k = 20; k < 26; ++k) w2dma(k, (k - 16) * 2048);
      waitv<20>();
    } else {
#pragma unroll
      for (int k = 0; k < 16; ++k) w2dma(k, k * 2048);
      waitv<12>();
#pragma unroll
      for (int k = 0; k < 10; ++k) { wr_[2 * k] = trd2(L, t2 + k * 2048, t2 + k * 2048 + 1024); wr_[2 * k + 1] = trd2(L, (t2 ^ 64u) + k * 2048, (t2 ^ 64u) + k * 2048 + 1024); }
      WAITL();
#pragma unroll
      for (int k = 16; k < 26; ++k) w2dma(k, (k - 16) * 2048);
      waitv<20>();
    }
    asm volatile("" : "+s"(lb), "+s"(w2b), "+s"(rot2));
#pragma unroll
    for (int k = 10; k < 12; ++k) { wr_[2 * k] = trd2(L, t2 + k * 2048, t2 + k * 2048 + 1024); wr_[2 * k + 1] = trd2(L, (t2 ^ 64u) + k * 2048, (t2 ^ 64u) + k * 2048 + 1024); }
    WAITL();
    w2dma(26, 10 * 2048); w2dma(27, 11 * 2048);
    lwait<1>(L, C_OK + 0, 1); lwait<1>(L, C_OK + 1, 1); lwait<1>(L, C_OK + 2, 1); lwait<1>(L, C_OK + 3, 1);
    STAMP(8);
    {
      const u32 fo = O_F2 + (dt * 28 + 7 * w + (lane < 7 ? lane : 6)) * 128;
      const v2u tg = *(LAS v2u*)(L + L_C + C_TAG * 4);
      _Pragma("unroll 1") for (u32 s = 0; s < SPIN_CAP; ++s) {
        asm volatile("" ::: "memory");
        v2u f;
        ld8s(f, fo, sw, 0);
        asm volatile("s_waitcnt vmcnt(0)" : "+v"(f)::"memory");
        if (__all(f.x == tg.x && f.y == tg.y)) break;
        __builtin_amdgcn_s_sleep(1);
      }
    }
    STAMP(9);
    auto uload = [&](int k) {
      u32 kg = k + rot2; kg = kg >= 28 ? kg - 28 : kg;
      const u32 so = O_U + (u32)(2 * dt * 112 + 28 * w + kg) * 1024u;
      ld16s(ring[2 * (k % RING)], lane * 16, sw, so);
      ld16s(ring[2 * (k % RING) + 1], lane * 16, sw, so + 112 * 1024);
    };
#pragma unroll
    for (int k = 0; k < RING; ++k) uload(k);
#pragma unroll
    for (int i = 0; i < 4; ++i)
#pragma unroll
      for (int j = 0; j < 16; ++j) acc[i][j] = 0.f;
    const int fs = 64 * st + ((tid >> 3) & 63);
    const u32 go = O_GA + fs * 1024 + dt * 128 + (tid & 7) * 16;
    {
      v4u wf[2][2];
      sfor<0, 28>([&](auto K) {
        constexpr int k = decltype(K)::v;
        waitv2<g2_after(k)>(ring[2 * (k % RING)], ring[2 * (k % RING) + 1]);
        if constexpr (k == 0) { STAMP(10); }
        constexpr int kn = k + 1;
        if constexpr (kn >= 12 && kn < 28) {
          constexpr u32 o = (kn < 16 ? kn : kn - 16) * 2048;
          wf[kn & 1][0] = trd2(L, t2 + o, t2 + o + 1024); wf[kn & 1][1] = trd2(L, (t2 ^ 64u) + o, (t2 ^ 64u) + o + 1024);
        }
        const v4u b0 = k < 12 ? wr_[(2 * k) % 24] : wf[k & 1][0];
        const v4u b1 = k < 12 ? wr_[(2 * k + 1) % 24] : wf[k & 1][1];
        acc[0] = mfma32(ring[2 * (k % RING)], b0, acc[0]);
        acc[1] = mfma32(ring[2 * (k % RING)], b1, acc[1]);
        acc[2] = mfma32(ring[2 * (k % RING) + 1], b0, acc[2]);
        acc[3] = mfma32(ring[2 * (k % RING) + 1], b1, acc[3]);
        SCHED();
        if constexpr (k + RING < 28) uload(k + RING);
        if constexpr (k == 22) {
          ld16s(fin_ga, go, sw, 0);
          asm volatile("buffer_load_dword %0, %1, %2, 0 offen sc1" : "=v"(fin_om) : "v"(O_OMG + fs * 4), "s"(sw) : "memory");
          const v4i sb = mksrd(p.b2, 2048 * 2);
          asm volatile("buffer_load_ushort %0, %1, %2, 0 offen" : "=v"(fin_b2) : "v"(fs * 2), "s"(sb) : "memory");
        }
        SCHED();
      });
    }
    STAMP(11);
#pragma unroll
    for (int md = 0; md < 2; ++md)
#pragma unroll
      for (int nt = 0; nt < 2; ++nt)
#pragma unroll
        for (int g = 0; g < 4; ++g) {
          const f16v& a = acc[2 * md + nt];
          const f4v v = {a[4 * g], a[4 * g + 1], a[4 * g + 2], a[4 * g + 3]};
          *(LAS f4v*)(L + lo + (32 * nt + (lane & 31)) * 272 + (32 * md + 8 * g + 4 * hh) * 4) = v;
        }
    asm volatile("s_waitcnt vmcnt(0)" : "+v"(fin_ga), "+v"(fin_om), "+v"(fin_b2)::"memory");
    _Pragma("unroll 1") for (u32 sp = 0; sp < SPIN_CAP && __any(max(max4(fin_ga), fin_om) == POISON); ++sp) {
      ld16s(fin_ga, go, sw, 0);
      asm volatile("buffer_load_dword %0, %1, %2, 0 offen sc1" : "=v"(fin_om) : "v"(O_OMG + fs * 4), "s"(sw) : "memory");
      asm volatile("s_waitcnt vmcnt(0)" : "+v"(fin_ga), "+v"(fin_om)::"memory");
    }
    STAMP(12);
  }
  WAITL();
  __builtin_amdgcn_s_barrier();
  asm volatile("" ::: "memory");
  {
    const int sl = tid >> 3;
    const u32 ro = L_G + sl * 272 + (tid & 7) * 32;
    f4v s0v = *(LAS f4v*)(L + ro), s1v = *(LAS f4v*)(L + ro + 16);
#pragma unroll
    for (int l = 1; l < 4; ++l) { s0v += *(LAS f4v*)(L + ro + l * 32768); s1v += *(LAS f4v*)(L + ro + l * 32768 + 16); }
    const float b2 = blo(fin_b2), om = __uint_as_float(fin_om);
    v4u o;
#pragma unroll
    for (int i = 0; i < 4; ++i) {
      const float e0 = i < 2 ? s0v[2 * i] : s1v[2 * i - 4], e1 = i < 2 ? s0v[2 * i + 1] : s1v[2 * i - 3];
      const u32 y = pk(e0, e1);
      const u32 yb = pk(blo(y) + b2, bhi(y) + b2);
      const u32 m = pk(om * blo(yb), om * bhi(yb));
      o[i] = pk(blo(fin_ga[i]) + blo(m), bhi(fin_ga[i]) + bhi(m));
    }
    *(v4u*)(p.out + (size_t)(64 * st + sl) * 512 + dt * 64 + (tid & 7) * 8) = o;
  }
  STAMP(15);
  const u64 clk1 = __builtin_amdgcn_s_memtime();
  if (tid == 0) ((u64*)(p.ws + O_DIAG))[256 * 128 + b] = clk1 - clk0;
  if (lane == 0) {
    u64* d = (u64*)(p.ws + O_DIAG) + (b * 8 + wv) * 16;
#pragma unroll
    for (int i = 0; i < 16; ++i) d[i] = ts[i];
  }
}

__global__ void pre_kernel(u64* d) { if (threadIdx.x == 0) d[0] = __builtin_amdgcn_s_memrealtime(); }
__global__ void amp_kernel(const u64* d, u64* sink) {
  const u64 me = __builtin_amdgcn_s_memrealtime();
  __shared__ u64 mn[256], mx[256];
  u64 a = ~0ull, z = 0;
  for (int w = 0; w < 8; ++w) { const u64* e = d + (threadIdx.x * 8 + w) * 16; a = a < e[0] ? a : e[0]; if (e[15] > z) z = e[15]; }
  mn[threadIdx.x] = a; mx[threadIdx.x] = z; __syncthreads();
  if (threadIdx.x == 0) { for (int i = 0; i < 256; ++i) { a = a < mn[i] ? a : mn[i]; z = z > mx[i] ? z : mx[i]; }
    u64 dur = AMP_MODE == 0 ? 0 : AMP_MODE == 1 ? (me - z) * 50 : (a - sink[8]) * 50; if (dur > 500000) dur = 500000; const u64 t0 = __builtin_amdgcn_s_memrealtime(); while (__builtin_amdgcn_s_memrealtime() - t0 < dur) __builtin_amdgcn_s_sleep(8); sink[0] = dur; }
}
extern "C" void kernel_launch(void* const* d_in, const int* in_sizes, int n_in, void* d_out, int out_size, void* d_ws, size_t ws_size,
                              hipStream_t stream) {
  Params p;
  p.x = (const u16*)d_in[0]; p.wr = (const u16*)d_in[1]; p.wi = (const u16*)d_in[2];
  p.w1 = (const u16*)d_in[4]; p.b1 = (const u16*)d_in[5]; p.w2 = (const u16*)d_in[6]; p.b2 = (const u16*)d_in[7]; p.gw = (const u16*)d_in[8];
  p.out = (u16*)d_out;
  p.ws = (char*)d_ws + ((ws_size - WS_BYTES) & ~(size_t)0xFFFFF);
  (void)hipFuncSetAttribute((const void*)fused_kernel, hipFuncAttributeMaxDynamicSharedMemorySize, LDS_BYTES);
  fused_kernel<<<256, 512, LDS_BYTES, stream>>>(p);
  amp_kernel<<<1, 256, 0, stream>>>((const u64*)(p.ws + O_DIAG), (u64*)(p.ws + O_DIAG + 600000));
}
```

```cpp
#define AMPM 0
#include <hip/hip_runtime.h>
#include <stdint.h>

typedef unsigned u32;
typedef unsigned long long u64;
typedef unsigned short u16;
typedef u32 v4u __attribute__((ext_vector_type(4)));
typedef u32 v2u __attribute__((ext_vector_type(2)));
typedef int v4i __attribute__((ext_vector_type(4)));
typedef short s4v __attribute__((ext_vector_type(4)));
typedef short s8v __attribute__((ext_vector_type(8)));
typedef float f2v __attribute__((ext_vector_type(2)));
typedef float f4v __attribute__((ext_vector_type(4)));
typedef float f16v __attribute__((ext_vector_type(16)));
typedef __bf16 b2v __attribute__((ext_vector_type(2)));
#define LAS __attribute__((address_space(3)))
#define DI __device__ __forceinline__
typedef __amdgpu_buffer_rsrc_t rsrc_t;

constexpr u32 O_EPOCH = 0, O_F1 = 4096, O_F2 = 32768, O_XN = 65536, O_VN = 73728, O_OMG = 81920, O_DIAG = 131072;
constexpr u32 O_H = 1u << 20, O_X1 = 3u << 20, O_V = 5u << 20, O_GA = 7u << 20, O_U = 9u << 20, WS_BYTES = 11u << 20;
constexpr u32 POISON = 0xFFFFFFFFu;
constexpr int AUX_SC1 = 16;
constexpr u32 SPIN_CAP = 200000;
constexpr u32 L_G = 0;
constexpr u32 L_S = 131072;
constexpr u32 L_X = L_S, L_HR = L_S + 8704, L_T = L_S + 17408;
constexpr u32 L_E = L_S, L_W = L_S + 8704, L_O = L_S + 17408;
constexpr u32 L_U = L_S;
constexpr u32 L_C = L_S + 26624;
constexpr u32 LDS_BYTES = 163840;
enum { C_H1 = 0, C_F1 = 1, C_OK = 2  , C_P2A = 6, C_P2B = 7, C_P3A = 8, C_P3B = 9, C_R1 = 10, C_R2 = 11, C_F2 = 12, C_TAG = 14  , C_G = 16  , C_NY = 24  , C_N = 32 };

struct Params {
  const u16 *x, *wr, *wi, *w1, *b1, *w2, *b2, *gw;
  u16* out;
  char* ws;
};

DI u32 pk(float a, float b) { f2v v = {a, b}; return __builtin_bit_cast(u32, __builtin_convertvector(v, b2v)); }
DI float blo(u32 v) { return __uint_as_float(v << 16); }
DI float bhi(u32 v) { return __uint_as_float(v & 0xFFFF0000u); }
DI float rbf(float a) { return blo(pk(a, a)); }
DI float cosr(int n, float inv) { return __builtin_amdgcn_cosf((float)n * inv); }
DI float sinr(int n, float inv) { return __builtin_amdgcn_sinf((float)n * inv); }
DI u32 san(u32 v) { return v < 0xFFFFFFFEu ? v : 0xFFFFFFFEu; }
DI v4u san4(v4u v) { v4u r = {san(v.x), san(v.y), san(v.z), san(v.w)}; return r; }
DI u32 max4(v4u v) { return max(max(v.x, v.y), max(v.z, v.w)); }
DI f4v mfma16(v4u a, v4u b, f4v c) { return __builtin_amdgcn_mfma_f32_16x16x32_bf16(__builtin_bit_cast(s8v, a), __builtin_bit_cast(s8v, b), c, 0, 0, 0); }
DI f16v mfma32(v4u a, v4u b, f16v c) { return __builtin_amdgcn_mfma_f32_32x32x16_bf16(__builtin_bit_cast(s8v, a), __builtin_bit_cast(s8v, b), c, 0, 0, 0); }
DI v2u trd(LAS char* L, u32 off) { return __builtin_bit_cast(v2u, __builtin_amdgcn_ds_read_tr16_b64_v4i16((LAS s4v*)(L + off))); }
DI v4u trd2(LAS char* L, u32 o0, u32 o1) { v2u a = trd(L, o0), b = trd(L, o1); v4u r = {a.x, a.y, b.x, b.y}; return r; }
template <int C> DI float dppadd(float v) { return v + __int_as_float(__builtin_amdgcn_update_dpp(0, __float_as_int(v), C, 0xF, 0xF, true)); }
DI float wsum(float v) {
  v = dppadd<0xB1>(v); v = dppadd<0x4E>(v); v = dppadd<0x141>(v); v = dppadd<0x140>(v);
  auto a = __builtin_amdgcn_permlane16_swap(__float_as_uint(v), __float_as_uint(v), false, false);
  v = __uint_as_float(a[0]) + __uint_as_float(a[1]);
  auto b = __builtin_amdgcn_permlane32_swap(__float_as_uint(v), __float_as_uint(v), false, false);
  return __uint_as_float(b[0]) + __uint_as_float(b[1]);
}
DI u64 mix64(u64 z) { z += 0x9E3779B97F4A7C15ull; z = (z ^ (z >> 30)) * 0xBF58476D1CE4E5B9ull; z = (z ^ (z >> 27)) * 0x94D049BB133111EBull; return z ^ (z >> 31); }
#define WAITL() asm volatile("s_waitcnt lgkmcnt(0)" ::: "memory")
template <int N> DI void waitv() { asm volatile("s_waitcnt vmcnt(%0)" ::"n"(N) : "memory"); }
#define SCHED() __builtin_amdgcn_sched_barrier(0)
DI u32 larrive(LAS char* L, int c, int lane) {
  WAITL();
  u32 old = 0;
  if (lane == 0) old = __hip_atomic_fetch_add((LAS u32*)(L + L_C) + c, 1u, __ATOMIC_RELAXED, __HIP_MEMORY_SCOPE_WORKGROUP);
  return __builtin_amdgcn_readfirstlane(old);
}
template <int SL = 0> DI void lwait(LAS char* L, int c, u32 n) {
  volatile LAS u32* p = (volatile LAS u32*)(L + L_C) + c;
  _Pragma("unroll 1") for (u32 s = 0; (u32)__builtin_amdgcn_readfirstlane(*p) < n && s < 40u * SPIN_CAP; ++s) { if (SL) __builtin_amdgcn_s_sleep(SL); }
  asm volatile("" ::: "memory");
}
DI void lsync(LAS char* L, int c, int lane) { larrive(L, c, lane); lwait(L, c, 4); }

#define STAMP(i)

template <int SGN> DI void dft8(const float (&ar)[8], const float (&ai)[8], float (&yr)[8], float (&yi)[8]) {
  const float R = 0.70710678118654752f, s = (float)SGN;
  float br[8], bi[8];
#pragma unroll
  for (int i = 0; i < 4; ++i) { br[i] = ar[i] + ar[i + 4]; bi[i] = ai[i] + ai[i + 4]; }
  br[4] = ar[0] - ar[4]; bi[4] = ai[0] - ai[4];
  { float r = ar[1] - ar[5], m = ai[1] - ai[5]; br[5] = R * (r - s * m); bi[5] = R * (m + s * r); }
  { float r = ar[2] - ar[6], m = ai[2] - ai[6]; br[6] = -s * m; bi[6] = s * r; }
  { float r = ar[3] - ar[7], m = ai[3] - ai[7]; br[7] = -R * (r + s * m); bi[7] = R * (s * r - m); }
#pragma unroll
  for (int h = 0; h < 2; ++h) {
    const int o = 4 * h;
    float er = br[o] + br[o + 2], ei = bi[o] + bi[o + 2], fr = br[o] - br[o + 2], fi = bi[o] - bi[o + 2];
    float gr = br[o + 1] + br[o + 3], gi = bi[o + 1] + bi[o + 3], hr = br[o + 1] - br[o + 3], hi = bi[o + 1] - bi[o + 3];
    yr[h] = er + gr; yi[h] = ei + gi; yr[h + 4] = er - gr; yi[h + 4] = ei - gi;
    yr[h + 2] = fr - s * hi; yi[h + 2] = fi + s * hr; yr[h + 6] = fr + s * hi; yi[h + 6] = fi - s * hr;
  }
}

struct P2C { v4u f1r, f1i, fr, fi, gr, gi, g1r, g1i; float tc[4], ts[4], uc[4], us[4]; };
DI v4u p2_column(const P2C& c, v4u in, v4u wv) {
  const f4v z = {0.f, 0.f, 0.f, 0.f};
  f4v r = mfma16(in, c.f1r, z), m = mfma16(in, c.f1i, z);
  v4u t;
#pragma unroll
  for (int i = 0; i < 4; ++i) t[i] = pk(r[i] * c.tc[i] + m[i] * c.ts[i], m[i] * c.tc[i] - r[i] * c.ts[i]);
  r = mfma16(c.fr, t, z); m = mfma16(c.fi, t, z);
#pragma unroll
  for (int i = 0; i < 4; ++i) { const float a = blo(wv[i]), b = bhi(wv[i]); t[i] = pk(r[i] * a - m[i] * b, r[i] * b + m[i] * a); }
  r = mfma16(t, c.gr, z); m = mfma16(t, c.gi, z);
#pragma unroll
  for (int i = 0; i < 4; ++i) t[i] = pk(r[i] * c.uc[i] - m[i] * c.us[i], m[i] * c.uc[i] + r[i] * c.us[i]);
  r = mfma16(c.g1r, t, z); m = mfma16(c.g1i, t, z);
#pragma unroll
  for (int i = 0; i < 4; ++i) t[i] = san(pk(r[i], m[i]));
  return t;
}

DI void dma16(u32 ldsaddr, u32 voff, v4i srd, u32 soff) {
  asm volatile("s_mov_b32 m0, %0\n\ts_nop 0\n\tbuffer_load_dwordx4 %1, %2, %3 offen lds" ::"s"(ldsaddr), "v"(voff), "s"(srd), "s"(soff) : "memory");
}
DI void ld16s(v4u& d, u32 voff, v4i srd, u32 soff) { asm volatile("buffer_load_dwordx4 %0, %1, %2, %3 offen sc1" : "=v"(d) : "v"(voff), "s"(srd), "s"(soff) : "memory"); }
DI void ld8s(v2u& d, u32 voff, v4i srd, u32 soff) { asm volatile("buffer_load_dwordx2 %0, %1, %2, %3 offen sc1" : "=v"(d) : "v"(voff), "s"(srd), "s"(soff) : "memory"); }
DI void ld8(v2u& d, u32 voff, v4i srd, u32 soff) { asm volatile("buffer_load_dwordx2 %0, %1, %2, %3 offen" : "=v"(d) : "v"(voff), "s"(srd), "s"(soff) : "memory"); }
DI void st16s(v4u d, u32 voff, v4i srd, u32 soff) { asm volatile("buffer_store_dwordx4 %0, %1, %2, %3 offen sc1\n\ts_nop 1" ::"v"(d), "v"(voff), "s"(srd), "s"(soff) : "memory"); }
DI void st8s(v2u d, u32 voff, v4i srd, u32 soff) { asm volatile("buffer_store_dwordx2 %0, %1, %2, %3 offen sc1\n\ts_nop 1" ::"v"(d), "v"(voff), "s"(srd), "s"(soff) : "memory"); }
template <int N> DI void waitv2(v4u& a, v4u& b) { asm volatile("s_waitcnt vmcnt(%2)" : "+v"(a), "+v"(b) : "n"(N) : "memory"); }
DI v4i mksrd(const void* p, u32 bytes) { const u64 a = (u64)p; v4i r = {(int)(u32)a, (int)((u32)(a >> 32) & 0xFFFFu), (int)bytes, 0x00020000}; return r; }

constexpr int RING = 6;
constexpr int g1_extra(int k) { return (k == 3 ? 8 : 0) + (k == 25 ? 4 : 0) + (k >= 26 && k <= 29 ? 4 : 0) + (k == 30 ? 4 : 0); }
constexpr int g1_after(int k) {
  int n = 0;
  if (k < RING) n += 2 * (RING - 1 - k);
  else n += g1_extra(k - RING);
  for (int j = (k < RING ? 0 : k - RING + 1); j < k; ++j) n += (j + RING < 32 ? 2 : 0) + g1_extra(j);
  return n;
}
constexpr int g2_extra(int k) { return k == 22 ? 3 : 0; }
constexpr int g2_after(int k) {
  int n = 0;
  if (k < RING) n += 2 * (RING - 1 - k);
  else n += g2_extra(k - RING);
  for (int j = (k < RING ? 0 : k - RING + 1); j < k; ++j) n += (j + RING < 28 ? 2 : 0) + g2_extra(j);
  return n;
}

template <int K> struct IC { static constexpr int v = K; };
template <int B, int E, class Fn> DI void sfor(Fn&& f) { if constexpr (B < E) { f(IC<B>{}); sfor<B + 1, E>(f); } }

__global__ void __launch_bounds__(512, 2) fused_kernel(Params p) {
  extern __shared__ __attribute__((aligned(16))) char smem[];
  LAS char* L = (LAS char*)smem;
  const int tid = threadIdx.x, lane = tid & 63, wv = __builtin_amdgcn_readfirstlane(tid >> 6), w = wv & 3;
  const int b = blockIdx.x, xl = b & 7, idx = b >> 3;
  const int dtl = idx & 3, dt = 4 * (xl >> 2) + dtl, grp = xl & 3;
  const int st = 8 * grp + (idx >> 2);
  STAMP(0);
  const rsrc_t rw = __builtin_amdgcn_make_buffer_rsrc((void*)p.ws, 0, (int)WS_BYTES, 0x00020000);
  asm volatile("" ::"s"(p.x), "s"(p.wr), "s"(p.wi), "s"(p.w1), "s"(p.b1), "s"(p.w2), "s"(p.b2), "s"(p.gw), "s"(p.out), "s"(p.ws));
  v4u fin_ga; u32 fin_om, fin_b2;

  if (wv < 4) {
    __builtin_amdgcn_s_setprio(3);
    const int s0 = b, q = lane >> 4, c = lane & 15;
    const rsrc_t rx = __builtin_amdgcn_make_buffer_rsrc((void*)p.x, 0, 2048 * 512 * 2, 0x00020000);
    const v4u x0 = __builtin_amdgcn_raw_buffer_load_b128(rx, lane * 16, (s0 + 512 * w) * 1024, 0);
    const v4u x1 = __builtin_amdgcn_raw_buffer_load_b128(rx, lane * 16, (s0 + 512 * w + 256) * 1024, 0);
    const v4u gwv = *(const v4u*)(p.gw + lane * 8);
    const v2u epv = __builtin_amdgcn_raw_buffer_load_b64(rw, O_EPOCH, 0, 0);
    {
      const v4u P = {POISON, POISON, POISON, POISON};
      __builtin_amdgcn_raw_buffer_store_b128(P, rw, lane * 16, O_X1 + (2 * w) * 262144 + s0 * 1024, AUX_SC1);
      __builtin_amdgcn_raw_buffer_store_b128(P, rw, lane * 16, O_X1 + (2 * w + 1) * 262144 + s0 * 1024, AUX_SC1);
      __builtin_amdgcn_raw_buffer_store_b128(P, rw, lane * 16, O_V + b * 8192 + w * 2048, AUX_SC1);
      __builtin_amdgcn_raw_buffer_store_b128(P, rw, lane * 16, O_V + b * 8192 + w * 2048 + 1024, AUX_SC1);
      __builtin_amdgcn_raw_buffer_store_b128(P, rw, lane * 16, O_GA + b * 8192 + w * 2048, AUX_SC1);
      __builtin_amdgcn_raw_buffer_store_b128(P, rw, lane * 16, O_GA + b * 8192 + w * 2048 + 1024, AUX_SC1);
      if (w == 0 && lane < 24)
        __builtin_amdgcn_raw_buffer_store_b32(POISON, rw, lane < 8 ? O_XN + lane * 1024 + s0 * 4 : (lane < 16 ? O_VN : O_OMG) + b * 32 + (lane & 7) * 4, 0, AUX_SC1);
    }
    if (tid < C_N) ((LAS u32*)(L + L_C))[tid] = 0;
    WAITL();
    __builtin_amdgcn_s_barrier();
    float xa[8], xb[8];
#pragma unroll
    for (int i = 0; i < 4; ++i) { xa[2 * i] = blo(x0[i]); xa[2 * i + 1] = bhi(x0[i]); xb[2 * i] = blo(x1[i]); xb[2 * i + 1] = bhi(x1[i]); }
    STAMP(1);
    *(LAS v4u*)(L + L_X + (2 * w) * 1088 + lane * 16) = x0;
    *(LAS v4u*)(L + L_X + (2 * w + 1) * 1088 + lane * 16) = x1;
    {
      float sa = ((xa[0] + xa[1]) + (xa[2] + xa[3])) + ((xa[4] + xa[5]) + (xa[6] + xa[7]));
      float sb = ((xb[0] + xb[1]) + (xb[2] + xb[3])) + ((xb[4] + xb[5]) + (xb[6] + xb[7]));
      const float ma = wsum(sa) * (1.f / 512.f), mb = wsum(sb) * (1.f / 512.f);
      float va = 0.f, vb = 0.f;
#pragma unroll
      for (int i = 0; i < 8; ++i) { const float da = xa[i] - ma, db = xb[i] - mb; va += da * da; vb += db * db; }
      va = wsum(va) * (1.f / 512.f); vb = wsum(vb) * (1.f / 512.f);
      const float ra = __builtin_amdgcn_rcpf(rbf(__builtin_amdgcn_sqrtf(rbf(rbf(va) + 1e-5f))));
      const float rb = __builtin_amdgcn_rcpf(rbf(__builtin_amdgcn_sqrtf(rbf(rbf(vb) + 1e-5f))));
      const float na = -rbf(ma) * ra, nb = -rbf(mb) * rb;
      v4u ha, hb;
#pragma unroll
      for (int i = 0; i < 4; ++i) {
        ha[i] = pk(xa[2 * i] * ra + na, xa[2 * i + 1] * ra + na);
        hb[i] = pk(xb[2 * i] * rb + nb, xb[2 * i + 1] * rb + nb);
      }
      *(LAS v4u*)(L + L_HR + (2 * w) * 1088 + lane * 16) = ha;
      *(LAS v4u*)(L + L_HR + (2 * w + 1) * 1088 + lane * 16) = hb;
    }
    lsync(L, C_H1, lane);
    STAMP(11);
    {
      const u32 ta = L_HR + (c >> 2) * 1088 + (128 * w + 16 * q + 4 * (c & 3)) * 2;
      const v4u h0 = trd2(L, ta, ta + 4 * 1088), h1 = trd2(L, ta + 128, ta + 128 + 4 * 1088);
      const u32 vo = (lane >> 5) * 131072 + (lane & 31) * 16;
      __builtin_amdgcn_raw_buffer_store_b128(h0, rw, vo, O_H + (4 * w) * 131072 + s0 * 512, AUX_SC1);
      __builtin_amdgcn_raw_buffer_store_b128(h1, rw, vo, O_H + (4 * w + 2) * 131072 + s0 * 512, AUX_SC1);
    }
    STAMP(2);
    float om_keep;
    {
      float ga = 0.f, gb = 0.f;
#pragma unroll
      for (int i = 0; i < 4; ++i) {
        const float g0 = blo(gwv[i]), g1 = bhi(gwv[i]);
        const u32 pa = pk(xa[2 * i] * g0, xa[2 * i + 1] * g1), pb = pk(xb[2 * i] * g0, xb[2 * i + 1] * g1);
        ga += blo(pa) + bhi(pa); gb += blo(pb) + bhi(pb);
      }
      ga = rbf(wsum(ga)); gb = rbf(wsum(gb));
      const float gs = (lane & 1) ? gb : ga;
      const float g = rbf(__builtin_amdgcn_rcpf(1.f + __builtin_amdgcn_exp2f(-1.44269504088896f * gs)));
      om_keep = rbf(1.f - g);
      if (lane < 2) ((LAS float*)(L + L_C))[C_G + 2 * w + lane] = g;
    }
    v4u f64r[2], f64i[2], a8;
    float twc[4], tws[4];
    {
      const int k1 = 16 * w + c;
#pragma unroll
      for (int ks = 0; ks < 2; ++ks)
#pragma unroll
        for (int j = 0; j < 4; ++j) {
          const int a = 32 * ks + 8 * q + 2 * j, n0 = (a * k1) & 63, n1 = ((a + 1) * k1) & 63;
          f64r[ks][j] = pk(cosr(n0, 1.f / 64), cosr(n1, 1.f / 64));
          f64i[ks][j] = pk(-sinr(n0, 1.f / 64), -sinr(n1, 1.f / 64));
        }
#pragma unroll
      for (int r = 0; r < 4; ++r) {
        const int bb = 4 * (q & 1) + r, n = (bb * (c >> 2)) & 7;
        const float cs = cosr(n, 0.125f), sn = sinr(n, 0.125f);
        const u32 e = (c & 1) ? pk(-sn, cs) : pk(cs, sn);
        a8[r] = (((c >> 1) & 1) == (q >> 1)) ? e : 0u;
        twc[r] = cosr(bb * k1, 1.f / 512); tws[r] = sinr(bb * k1, 1.f / 512);
      }
    }
    const u64 tag = mix64(((u64)(u32)__builtin_amdgcn_readfirstlane(epv.y) << 32) | (u32)__builtin_amdgcn_readfirstlane(epv.x));
    if (lane == 0) { const v2u tg = {(u32)tag, (u32)(tag >> 32)}; *(LAS v2u*)(L + L_C + C_TAG * 4) = tg; }
    STAMP(12);
    waitv<0>();
    STAMP(3);
    if (larrive(L, C_F1, lane) == 3 && lane < 8) {
      const v2u tg = {(u32)tag, (u32)(tag >> 32)};
      __builtin_amdgcn_raw_buffer_store_b64(tg, rw, O_F1 + lane * 2048 + b * 8, 0, AUX_SC1);
    }
    const int j1 = xl, kb = idx;
    v4u wq0, wq1;
    {
      const rsrc_t rr = __builtin_amdgcn_make_buffer_rsrc((void*)p.wr, 0, 2048 * 257 * 2, 0x00020000);
      const rsrc_t ri = __builtin_amdgcn_make_buffer_rsrc((void*)p.wi, 0, 2048 * 257 * 2, 0x00020000);
      const u32 o = (u32)(j1 + 8 * (64 * w + lane)) * 514u + kb * 16;
      wq0 = __builtin_amdgcn_raw_buffer_load_b128(rr, o, 0, 0);
      wq1 = __builtin_amdgcn_raw_buffer_load_b128(ri, o, 0, 0);
    }
    {
      float yr[8], yi[8], nr[8], ni[8];
      const f4v z = {0.f, 0.f, 0.f, 0.f};
      const u32 xo = L_X + ((c >> 1) & 1) * 1088 + (8 * q + (c >> 2)) * 16 + 8 * (c & 1);
      v4u a8n = a8; a8n[1] ^= 0x80008000u; a8n[3] ^= 0x80008000u;
#pragma unroll
      for (int pi = 0; pi < 4; ++pi) {
        const v4u A0 = trd2(L, xo + pi * 2176, xo + pi * 2176 + 64), A1 = trd2(L, xo + pi * 2176 + 512, xo + pi * 2176 + 576);
        f4v r = mfma16(A0, f64r[0], z), m = mfma16(A0, f64i[0], z);
        r = mfma16(A1, f64r[1], r); m = mfma16(A1, f64i[1], m);
        v4u t;
#pragma unroll
        for (int i = 0; i < 4; ++i) t[i] = pk(r[i] * twc[i] + m[i] * tws[i], m[i] * twc[i] - r[i] * tws[i]);
        const f4v d = mfma16(a8, t, z);
        yr[2 * pi] = d[0]; yi[2 * pi] = d[1]; yr[2 * pi + 1] = d[2]; yi[2 * pi + 1] = d[3];
        if (w == 0) { const f4v e = mfma16(a8n, t, z); nr[2 * pi] = e[0]; ni[2 * pi] = e[1]; nr[2 * pi + 1] = e[2]; ni[2 * pi + 1] = e[3]; }
      }
      float zr[8], zi[8];
      dft8<-1>(yr, yi, zr, zi);
#pragma unroll
      for (int j = 0; j < 8; ++j) *(LAS u32*)(L + L_T + w * 2048 + j * 256 + lane * 4) = san(pk(zr[j], zi[j]));
      const v4u o0 = *(LAS v4u*)(L + L_T + w * 2048 + lane * 16), o1 = *(LAS v4u*)(L + L_T + w * 2048 + 1024 + lane * 16);
      STAMP(13);
      lwait(L, C_F1, 4);
      const u32 vo = q * 262144 + (((lane >> 2) & 3) * 64 + (lane & 3) * 4) * 4;
      __builtin_amdgcn_raw_buffer_store_b128(o0, rw, vo, O_X1 + s0 * 1024 + w * 64, AUX_SC1);
      __builtin_amdgcn_raw_buffer_store_b128(o1, rw, vo, O_X1 + 4 * 262144 + s0 * 1024 + w * 64, AUX_SC1);
      if (w == 0) {
        dft8<-1>(nr, ni, zr, zi);
        if (lane == 0) {
#pragma unroll
          for (int j = 0; j < 8; ++j) __builtin_amdgcn_raw_buffer_store_b32(san(pk(zr[j], zi[j])), rw, O_XN + j * 1024 + s0 * 4, 0, AUX_SC1);
        }
      }
    }
    STAMP(4);
    const u32 epos = ((w * 16 + c) * 4 + q) * 4;
    P2C pc;
    {
#pragma unroll
      for (int i = 0; i < 4; ++i) {
        const int e = 4 * q + i;
        { const int n = (e * (8 * c + j1)) & 127; const float cs = cosr(n, 1.f / 128), sn = sinr(n, 1.f / 128); pc.f1r[i] = pk(cs, sn); pc.f1i[i] = pk(-sn, cs); }
        { const int n = (e * c) & 15; const float cs = cosr(n, 1.f / 16), sn = sinr(n, 1.f / 16); pc.fr[i] = pk(cs, sn); pc.fi[i] = pk(-sn, cs); pc.gr[i] = pk(cs, -sn); pc.gi[i] = pk(sn, cs); }
        { const int n = (c * (8 * e + j1)) & 127; const float cs = cosr(n, 1.f / 128), sn = sinr(n, 1.f / 128); pc.g1r[i] = pk(cs, -sn); pc.g1i[i] = pk(sn, cs); }
        pc.tc[i] = cosr(e * (8 * c + j1), 1.f / 2048); pc.ts[i] = sinr(e * (8 * c + j1), 1.f / 2048);
        pc.uc[i] = cosr(c * (8 * e + j1), 1.f / 2048); pc.us[i] = sinr(c * (8 * e + j1), 1.f / 2048);
      }
#pragma unroll
      for (int i = 0; i < 4; ++i) {
        u32 lo = __builtin_amdgcn_perm(wq1[i], wq0[i], 0x05040100u), hi = __builtin_amdgcn_perm(wq1[i], wq0[i], 0x07060302u);
        if (i == 0 && kb == 0) lo = pk(0.5f * blo(lo), 0.5f * bhi(lo));
        *(LAS u32*)(L + L_W + (2 * i) * 1024 + epos) = lo;
        *(LAS u32*)(L + L_W + (2 * i + 1) * 1024 + epos) = hi;
      }
    }
    {
      const u32 fo = O_F1 + xl * 2048 + (64 * w + lane) * 8;
      _Pragma("unroll 1") for (u32 s = 0; s < SPIN_CAP; ++s) {
        asm volatile("" ::: "memory");
        const v2u f = __builtin_amdgcn_raw_buffer_load_b64(rw, fo, 0, AUX_SC1);
        if (__all(f.x == (u32)tag && f.y == (u32)(tag >> 32))) break;
        __builtin_amdgcn_s_sleep(2);
      }
      asm volatile("" ::: "memory");
      if (lane == 0) { ((volatile LAS u32*)(L + L_C))[C_OK + w] = 1u; }
      STAMP(5);
      lwait(L, C_OK + 0, 1); lwait(L, C_OK + 1, 1); lwait(L, C_OK + 2, 1); lwait(L, C_OK + 3, 1);
    }
    if (b == 0 && tid == 0) *(u64*)(p.ws + O_EPOCH) = tag;
    if (lane < 2) __builtin_amdgcn_raw_buffer_store_b32(__float_as_uint(om_keep), rw, O_OMG + (s0 + 256 * (2 * w + lane)) * 4, 0, AUX_SC1);
    {
      const u32 xo = O_X1 + j1 * 262144 + (64 * w + lane) * 1024 + kb * 32;
      v4u e0 = __builtin_amdgcn_raw_buffer_load_b128(rw, xo, 0, AUX_SC1), e1 = __builtin_amdgcn_raw_buffer_load_b128(rw, xo + 16, 0, AUX_SC1);
      _Pragma("unroll 1") for (u32 s = 0; s < SPIN_CAP; ++s) {
        asm volatile("" ::: "memory");
        const bool bad = max(max4(e0), max4(e1)) == POISON;
        if (!__any(bad)) break;
        __builtin_amdgcn_s_sleep(1);
        if (bad) { e0 = __builtin_amdgcn_raw_buffer_load_b128(rw, xo, 0, AUX_SC1); e1 = __builtin_amdgcn_raw_buffer_load_b128(rw, xo + 16, 0, AUX_SC1); }
      }
      STAMP(6);
#pragma unroll
      for (int i = 0; i < 4; ++i) { *(LAS u32*)(L + L_E + i * 1024 + epos) = e0[i]; *(LAS u32*)(L + L_E + (4 + i) * 1024 + epos) = e1[i]; }
      lsync(L, C_P2A, lane);
      const v4u i0 = *(LAS v4u*)(L + L_E + (2 * w) * 1024 + lane * 16), i1 = *(LAS v4u*)(L + L_E + (2 * w + 1) * 1024 + lane * 16);
      const v4u w0 = *(LAS v4u*)(L + L_W + (2 * w) * 1024 + lane * 16), w1 = *(LAS v4u*)(L + L_W + (2 * w + 1) * 1024 + lane * 16);
      *(LAS v4u*)(L + L_O + (2 * w) * 1024 + lane * 16) = p2_column(pc, i0, w0);
      *(LAS v4u*)(L + L_O + (2 * w + 1) * 1024 + lane * 16) = p2_column(pc, i1, w1);
      lsync(L, C_P2B, lane);
      v4u o0, o1;
#pragma unroll
      for (int i = 0; i < 4; ++i) { o0[i] = *(LAS u32*)(L + L_O + i * 1024 + epos); o1[i] = *(LAS u32*)(L + L_O + (4 + i) * 1024 + epos); }
      STAMP(14);
      const u32 vo = O_V + (64 * w + lane) * 8192 + j1 * 1024 + kb * 32;
      __builtin_amdgcn_raw_buffer_store_b128(o0, rw, vo, 0, AUX_SC1);
      __builtin_amdgcn_raw_buffer_store_b128(o1, rw, vo + 16, 0, AUX_SC1);
      if (kb == 31 && w == 0) {
        v4u in, wn;
#pragma unroll
        for (int i = 0; i < 4; ++i) {
          const u32 e = 64 * q + 16 * i + c, o = (j1 + 8 * e) * 257 + 256;
          wn[i] = pk(0.5f * blo(p.wr[o]), 0.5f * blo(p.wi[o]));
          in[i] = POISON;
        }
        _Pragma("unroll 1") for (u32 s = 0; s < SPIN_CAP; ++s) {
        asm volatile("" ::: "memory");
#pragma unroll
          for (int i = 0; i < 4; ++i) in[i] = __builtin_amdgcn_raw_buffer_load_b32(rw, O_XN + j1 * 1024 + (64 * q + 16 * i + c) * 4, 0, AUX_SC1);
          if (!__any(max4(in) == POISON)) break;
        }
        const v4u o = p2_column(pc, in, wn);
#pragma unroll
        for (int i = 0; i < 4; ++i) __builtin_amdgcn_raw_buffer_store_b32(o[i], rw, O_VN + (64 * q + 16 * i + c) * 32 + j1 * 4, 0, AUX_SC1);
      }
    }
    STAMP(7);
    {
      v4u gB[4];
      float pcs[2], psn[2];
      u32 ax;
      {
        const int ap = 16 * w + c;
#pragma unroll
        for (int ks = 0; ks < 4; ++ks)
#pragma unroll
          for (int i = 0; i < 4; ++i) {
            const int n = ((16 * ks + 4 * q + i) * ap) & 63;
            gB[ks][i] = pk(cosr(n, 1.f / 64) * 0x1p-19f, -sinr(n, 1.f / 64) * 0x1p-19f);
          }
        const int n = (q * (c >> 1)) & 7;
        const float cs = cosr(n, 0.125f), sn = sinr(n, 0.125f);
        ax = (c & 1) ? pk(sn, cs) : pk(cs, -sn);
#pragma unroll
        for (int e = 0; e < 2; ++e) { pcs[e] = cosr(ap * (2 * q + e), 1.f / 512); psn[e] = sinr(ap * (2 * q + e), 1.f / 512); }
      }
      const u32 vo = O_V + s0 * 8192 + (16 * w + 64 * q + c) * 4;
      u32 vv[8];
#pragma unroll
      for (int j = 0; j < 8; ++j) vv[j] = __builtin_amdgcn_raw_buffer_load_b32(rw, vo + j * 1024, 0, AUX_SC1);
      _Pragma("unroll 1") for (u32 s = 0; s < SPIN_CAP; ++s) {
        asm volatile("" ::: "memory");
        const bool bad = max(max(max(vv[0], vv[1]), max(vv[2], vv[3])), max(max(vv[4], vv[5]), max(vv[6], vv[7]))) == POISON;
        if (!__any(bad)) break;
        __builtin_amdgcn_s_sleep(1);
        if (bad) {
#pragma unroll
          for (int j = 0; j < 8; ++j) vv[j] = __builtin_amdgcn_raw_buffer_load_b32(rw, vo + j * 1024, 0, AUX_SC1);
        }
      }
      STAMP(8);
      float ar[8], ai[8], yr[8], yi[8];
#pragma unroll
      for (int j = 0; j < 8; ++j) { ar[j] = blo(vv[j]); ai[j] = bhi(vv[j]); }
      dft8<1>(ar, ai, yr, yi);
      v4u yq[2];
#pragma unroll
      for (int i = 0; i < 8; ++i) yq[i >> 2][i & 3] = pk(yr[i], yi[i]);
      const f4v z = {0.f, 0.f, 0.f, 0.f};
#pragma unroll
      for (int i = 0; i < 8; ++i) {
        v4u A = {0u, 0u, 0u, 0u};
        A[i & 3] = ax;
        const f4v d = mfma16(A, yq[i >> 2], z);
        *(LAS u32*)(L + L_U + i * 2176 + (2 * q) * 272 + (16 * w + c) * 4) = pk(d[0] * pcs[0] - d[1] * psn[0], d[1] * pcs[0] + d[0] * psn[0]);
        *(LAS u32*)(L + L_U + i * 2176 + (2 * q + 1) * 272 + (16 * w + c) * 4) = pk(d[2] * pcs[1] - d[3] * psn[1], d[3] * pcs[1] + d[2] * psn[1]);
      }
      if (w == 0) {
        v4u n0, n1;
        _Pragma("unroll 1") for (u32 s = 0; s < SPIN_CAP; ++s) {
        asm volatile("" ::: "memory");
          n0 = __builtin_amdgcn_raw_buffer_load_b128(rw, O_VN + s0 * 32, 0, AUX_SC1);
          n1 = __builtin_amdgcn_raw_buffer_load_b128(rw, O_VN + s0 * 32 + 16, 0, AUX_SC1);
          if (!__any(max(max4(n0), max4(n1)) == POISON)) break;
        }
#pragma unroll
        for (int j = 0; j < 4; ++j) { ar[j] = blo(n0[j]); ai[j] = bhi(n0[j]); ar[j + 4] = blo(n1[j]); ai[j + 4] = bhi(n1[j]); }
        dft8<1>(ar, ai, yr, yi);
        if (lane == 0) {
#pragma unroll
          for (int i = 0; i < 8; ++i) ((LAS float*)(L + L_C))[C_NY + i] = yr[i] * 0x1p-19f;
        }
      }
      lsync(L, C_P3B, lane);
      const u32 uo = L_U + (c >> 3) * 2176 + (c & 7) * 272 + q * 16;
#pragma unroll
      for (int pi = 0; pi < 4; ++pi) {
        f4v d = z;
#pragma unroll
        for (int ks = 0; ks < 4; ++ks) d = mfma16(*(LAS v4u*)(L + uo + pi * 4352 + ks * 64), gB[ks], d);
        const int i = 2 * pi + (q >> 1);
        const float ny = ((LAS float*)(L + L_C))[C_NY + i], g = ((LAS float*)(L + L_C))[C_G + i];
        const u32 a01 = pk(d[0] + ny, d[1] - ny), a23 = pk(d[2] + ny, d[3] - ny);
        const v2u o = {san(pk(g * blo(a01), g * bhi(a01))), san(pk(g * blo(a23), g * bhi(a23)))};
        __builtin_amdgcn_raw_buffer_store_b64(o, rw, O_GA + (s0 + 256 * i) * 1024 + ((16 * w + c) * 8 + 4 * (q & 1)) * 2, 0, AUX_SC1);
      }
    }
    STAMP(9);
    {
      const int sl = (tid >> 3) & 63, s = 64 * st + sl;
      fin_b2 = p.b2[s];
      const u32 go = O_GA + s * 1024 + dt * 128 + (tid & 7) * 16;
      _Pragma("unroll 1") for (u32 sp = 0; sp < SPIN_CAP; ++sp) {
        asm volatile("" ::: "memory");
        fin_ga = __builtin_amdgcn_raw_buffer_load_b128(rw, go, 0, AUX_SC1);
        fin_om = __builtin_amdgcn_raw_buffer_load_b32(rw, O_OMG + s * 4, 0, AUX_SC1);
        if (!__any(max(max4(fin_ga), fin_om) == POISON)) break;
        __builtin_amdgcn_s_sleep(4);
      }
    }
    STAMP(10);
  } else {
    __builtin_amdgcn_s_barrier();
    const v4i sw = mksrd(p.ws, WS_BYTES), s2 = mksrd(p.w2, 1792u * 4096u);
    u32 lb = (u32)(size_t)smem + L_G + w * 32768, w2b = (u32)(448 * w) * 4096u + st * 128, rot2 = 7 * dtl;
    const u32 lo = L_G + w * 32768;
    const int hh = lane >> 5, blk = (lane >> 4) & 1, tq = (lane & 15) >> 2, tp = lane & 3;
    const u32 v2off = (lane >> 3) * 4096 + (((lane & 7) ^ (((lane >> 4) & 1) << 2)) * 16);
    auto w2dma = [&](int kl, u32 dst) {
      u32 kg = kl + rot2; kg = kg >= 28 ? kg - 28 : kg;
      const u32 so = w2b + kg * 65536u;
      dma16(lb + dst, v2off, s2, so);
      dma16(lb + dst + 1024, v2off, s2, so + 8 * 4096);
    };
    const u32 t2 = lo + (4 * hh + tq) * 128 + (((2 * blk + (tp >> 1)) ^ (((tq >> 1) & 1) << 2)) * 16) + 8 * (tp & 1);
    v4u wr_[24];
    f16v acc[4];
    v4u ring[2 * RING];
    if (idx < 28) {
      const int ft = 7 * grp + (idx >> 2);
      const v4i s1 = mksrd(p.w1, 2048u * 3584u);
      u32 v1off[4];
#pragma unroll
      for (int j = 0; j < 4; ++j) v1off[j] = ((lane >> 3) ^ (j >> 1)) * 3584 + ((lane & 7) ^ ((j & 1) << 2)) * 16;
      auto w1dma = [&](int bl, int slot) {
        const u32 so = (u32)(64 * w + 8 * ((bl + 2 * dtl) & 7)) * 3584u + ft * 128;
#pragma unroll
        for (int j = 0; j < 8; ++j) dma16(lb + slot * 8192 + j * 1024, v1off[j & 3], s1, so + j * (256u * 3584u));
      };
      const u32 t1 = lo + tq * 1024 + ((hh ^ (tq >> 1)) * 128) + ((((w >> 1) * 4 + 2 * blk + (tp >> 1)) ^ ((tq & 1) << 2)) * 16) + 8 * (tp & 1);
      const u32 t1x = t1 ^ 64u;
      w1dma(0, 0); w1dma(1, 1); STAMP(13); w1dma(2, 2); w1dma(3, 3);
      STAMP(1);
      sfor<0, 3>([&](auto B) {
        constexpr int bl = decltype(B)::v;
        waitv<24>();
#pragma unroll
        for (int k = 0; k < 4; ++k) {
          wr_[8 * bl + 2 * k] = trd2(L, t1 + bl * 8192 + k * 256, t1 + bl * 8192 + k * 256 + 4096);
          wr_[8 * bl + 2 * k + 1] = trd2(L, t1x + bl * 8192 + k * 256, t1x + bl * 8192 + k * 256 + 4096);
        }
        WAITL();
        w1dma(4 + bl, bl);
      });
      STAMP(2);
      lwait<1>(L, C_OK + w, 1);
      STAMP(3);
      auto hload = [&](int k) {
        const int pos = k >> 2, bl = pos < 4 ? pos + 3 : (pos < 7 ? pos - 4 : 7);
        const u32 so = O_H + (u32)(64 * w + 8 * ((bl + 2 * dtl) & 7) + 2 * (k & 3)) * 512u;
        ld16s(ring[2 * (k % RING)], lane * 16, sw, so + (2 * dt + (w & 1)) * 131072);
        ld16s(ring[2 * (k % RING) + 1], lane * 16, sw, so + (2 * dt + ((w & 1) ^ 1)) * 131072);
      };
#pragma unroll
      for (int k = 0; k < RING; ++k) hload(k);
#pragma unroll
      for (int i = 0; i < 4; ++i)
#pragma unroll
        for (int j = 0; j < 16; ++j) acc[i][j] = 0.f;
      v2u bias[4];
      v4u wf[2][2];
      sfor<0, 32>([&](auto K) {
        constexpr int k = decltype(K)::v, pos = k >> 2;
        waitv2<g1_after(k)>(ring[2 * (k % RING)], ring[2 * (k % RING) + 1]);
        if constexpr (k == 0) {
          STAMP(4);
          wf[0][0] = trd2(L, t1 + 3 * 8192, t1 + 3 * 8192 + 4096); wf[0][1] = trd2(L, t1x + 3 * 8192, t1x + 3 * 8192 + 4096);
        }
        constexpr int kn = k + 1, pn = kn >> 2;
        if constexpr (kn < 32 && (pn < 4 || pn == 7)) {
          constexpr u32 o = (pn == 0 || pn == 7 ? 3 : pn - 1) * 8192 + (kn & 3) * 256;
          wf[kn & 1][0] = trd2(L, t1 + o, t1 + o + 4096); wf[kn & 1][1] = trd2(L, t1x + o, t1x + o + 4096);
        }
        const v4u a0 = (pos >= 4 && pos < 7) ? wr_[2 * ((k - 16) & 15) % 24] : wf[k & 1][0];
        const v4u a1 = (pos >= 4 && pos < 7) ? wr_[(2 * ((k - 16) & 15) + 1) % 24] : wf[k & 1][1];
        acc[0] = mfma32(a0, ring[2 * (k % RING)], acc[0]);
        acc[1] = mfma32(a0, ring[2 * (k % RING) + 1], acc[1]);
        acc[2] = mfma32(a1, ring[2 * (k % RING)], acc[2]);
        acc[3] = mfma32(a1, ring[2 * (k % RING) + 1], acc[3]);
        SCHED();
        if constexpr (k + RING < 32) hload(k + RING);
        if constexpr (k == 15) { STAMP(14); }
        if constexpr (k == 3) { WAITL(); w1dma(7, 3); }
        if constexpr (k == 25) {
          const v4i sb = mksrd(p.b1, 1792 * 2);
#pragma unroll
          for (int g = 0; g < 4; ++g) ld8(bias[g], (8 * g + 4 * hh) * 2, sb, (64 * ft + 32 * (w >> 1)) * 2);
        }
        if constexpr (k >= 26 && k <= 30) { w2dma(2 * (k - 26), 2 * (k - 26) * 2048); w2dma(2 * (k - 26) + 1, (2 * (k - 26) + 1) * 2048); }
        SCHED();
      });
      STAMP(5);
#pragma unroll
      for (int l = 1; l < 4; ++l)
#pragma unroll
        for (int g = 0; g < 4; ++g) {
          const f4v v = {acc[l][4 * g], acc[l][4 * g + 1], acc[l][4 * g + 2], acc[l][4 * g + 3]};
          *(LAS f4v*)(L + lo + 20480 + (l - 1) * 4096 + g * 1024 + lane * 16) = v;
        }
      lsync(L, C_R1, lane);
#pragma unroll
      for (int l = 1; l < 4; ++l)
#pragma unroll
        for (int g = 0; g < 4; ++g) {
          const f4v v = *(LAS f4v*)(L + L_G + (w ^ l) * 32768 + 20480 + (l - 1) * 4096 + g * 1024 + lane * 16);
#pragma unroll
          for (int e = 0; e < 4; ++e) acc[0][4 * g + e] += v[e];
        }
      larrive(L, C_R2, lane);
      asm volatile("s_waitcnt vmcnt(20)" : "+v"(bias[0]), "+v"(bias[1]), "+v"(bias[2]), "+v"(bias[3])::"memory");
      v4u uf[2];
#pragma unroll
      for (int g = 0; g < 4; ++g) {
        float o[4];
#pragma unroll
        for (int e = 0; e < 4; ++e) {
          const float v = acc[0][4 * g + e] + ((e & 1) ? bhi(bias[g][e >> 1]) : blo(bias[g][e >> 1]));
          const float t = v * (-2.3022081985f - 0.1029432397f * v * v);
          o[e] = v * __builtin_amdgcn_rcpf(1.f + __builtin_amdgcn_exp2f(t));
        }
        uf[g >> 1][2 * (g & 1)] = pk(o[0], o[1]); uf[g >> 1][2 * (g & 1) + 1] = pk(o[2], o[3]);
      }
      const u32 uo = O_U + (u32)((2 * dt + (w & 1)) * 112 + 4 * ft + 2 * (w >> 1)) * 1024u;
      st16s(uf[0], lane * 16, sw, uo);
      st16s(uf[1], lane * 16, sw, uo + 1024);
      STAMP(6);
      waitv<2>();
#pragma unroll
      for (int k = 0; k < 10; ++k) { wr_[2 * k] = trd2(L, t2 + k * 2048, t2 + k * 2048 + 1024); wr_[2 * k + 1] = trd2(L, (t2 ^ 64u) + k * 2048, (t2 ^ 64u) + k * 2048 + 1024); }
      WAITL();
#pragma unroll
      for (int k = 16; k < 20; ++k) w2dma(k, (k - 16) * 2048);
      waitv<8>();
      if (larrive(L, C_F2, lane) == 3 && lane == 0) st8s(*(LAS v2u*)(L + L_C + C_TAG * 4), 0, sw, O_F2 + (dt * 28 + ft) * 128);
      STAMP(7);
      lwait(L, C_R2, 4);
#pragma unroll
      for (int k = 10; k < 16; ++k) w2dma(k, k * 2048);
#pragma unroll
      for (int k = 20; k < 26; ++k) w2dma(k, (k - 16) * 2048);
      waitv<20>();
    } else {
#pragma unroll
      for (int k = 0; k < 16; ++k) w2dma(k, k * 2048);
      waitv<12>();
#pragma unroll
      for (int k = 0; k < 10; ++k) { wr_[2 * k] = trd2(L, t2 + k * 2048, t2 + k * 2048 + 1024); wr_[2 * k + 1] = trd2(L, (t2 ^ 64u) + k * 2048, (t2 ^ 64u) + k * 2048 + 1024); }
      WAITL();
#pragma unroll
      for (int k = 16; k < 26; ++k) w2dma(k, (k - 16) * 2048);
      waitv<20>();
    }
    asm volatile("" : "+s"(lb), "+s"(w2b), "+s"(rot2));
#pragma unroll
    for (int k = 10; k < 12; ++k) { wr_[2 * k] = trd2(L, t2 + k * 2048, t2 + k * 2048 + 1024); wr_[2 * k + 1] = trd2(L, (t2 ^ 64u) + k * 2048, (t2 ^ 64u) + k * 2048 + 1024); }
    WAITL();
    w2dma(26, 10 * 2048); w2dma(27, 11 * 2048);
    lwait<1>(L, C_OK + 0, 1); lwait<1>(L, C_OK + 1, 1); lwait<1>(L, C_OK + 2, 1); lwait<1>(L, C_OK + 3, 1);
    STAMP(8);
    {
      const u32 fo = O_F2 + (dt * 28 + 7 * w + (lane < 7 ? lane : 6)) * 128;
      const v2u tg = *(LAS v2u*)(L + L_C + C_TAG * 4);
      _Pragma("unroll 1") for (u32 s = 0; s < SPIN_CAP; ++s) {
        asm volatile("" ::: "memory");
        v2u f;
        ld8s(f, fo, sw, 0);
        asm volatile("s_waitcnt vmcnt(0)" : "+v"(f)::"memory");
        if (__all(f.x == tg.x && f.y == tg.y)) break;
        __builtin_amdgcn_s_sleep(1);
      }
    }
    STAMP(9);
    auto uload = [&](int k) {
      u32 kg = k + rot2; kg = kg >= 28 ? kg - 28 : kg;
      const u32 so = O_U + (u32)(2 * dt * 112 + 28 * w + kg) * 1024u;
      ld16s(ring[2 * (k % RING)], lane * 16, sw, so);
      ld16s(ring[2 * (k % RING) + 1], lane * 16, sw, so + 112 * 1024);
    };
#pragma unroll
    for (int k = 0; k < RING; ++k) uload(k);
#pragma unroll
    for (int i = 0; i < 4; ++i)
#pragma unroll
      for (int j = 0; j < 16; ++j) acc[i][j] = 0.f;
    const int fs = 64 * st + ((tid >> 3) & 63);
    const u32 go = O_GA + fs * 1024 + dt * 128 + (tid & 7) * 16;
    {
      v4u wf[2][2];
      sfor<0, 28>([&](auto K) {
        constexpr int k = decltype(K)::v;
        waitv2<g2_after(k)>(ring[2 * (k % RING)], ring[2 * (k % RING) + 1]);
        if constexpr (k == 0) { STAMP(10); }
        constexpr int kn = k + 1;
        if constexpr (kn >= 12 && kn < 28) {
          constexpr u32 o = (kn < 16 ? kn : kn - 16) * 2048;
          wf[kn & 1][0] = trd2(L, t2 + o, t2 + o + 1024); wf[kn & 1][1] = trd2(L, (t2 ^ 64u) + o, (t2 ^ 64u) + o + 1024);
        }
        const v4u b0 = k < 12 ? wr_[(2 * k) % 24] : wf[k & 1][0];
        const v4u b1 = k < 12 ? wr_[(2 * k + 1) % 24] : wf[k & 1][1];
        acc[0] = mfma32(ring[2 * (k % RING)], b0, acc[0]);
        acc[1] = mfma32(ring[2 * (k % RING)], b1, acc[1]);
        acc[2] = mfma32(ring[2 * (k % RING) + 1], b0, acc[2]);
        acc[3] = mfma32(ring[2 * (k % RING) + 1], b1, acc[3]);
        SCHED();
        if constexpr (k + RING < 28) uload(k + RING);
        if constexpr (k == 22) {
          ld16s(fin_ga, go, sw, 0);
          asm volatile("buffer_load_dword %0, %1, %2, 0 offen sc1" : "=v"(fin_om) : "v"(O_OMG + fs * 4), "s"(sw) : "memory");
          const v4i sb = mksrd(p.b2, 2048 * 2);
          asm volatile("buffer_load_ushort %0, %1, %2, 0 offen" : "=v"(fin_b2) : "v"(fs * 2), "s"(sb) : "memory");
        }
        SCHED();
      });
    }
    STAMP(11);
#pragma unroll
    for (int md = 0; md < 2; ++md)
#pragma unroll
      for (int nt = 0; nt < 2; ++nt)
#pragma unroll
        for (int g = 0; g < 4; ++g) {
          const f16v& a = acc[2 * md + nt];
          const f4v v = {a[4 * g], a[4 * g + 1], a[4 * g + 2], a[4 * g + 3]};
          *(LAS f4v*)(L + lo + (32 * nt + (lane & 31)) * 272 + (32 * md + 8 * g + 4 * hh) * 4) = v;
        }
    asm volatile("s_waitcnt vmcnt(0)" : "+v"(fin_ga), "+v"(fin_om), "+v"(fin_b2)::"memory");
    _Pragma("unroll 1") for (u32 sp = 0; sp < SPIN_CAP && __any(max(max4(fin_ga), fin_om) == POISON); ++sp) {
      ld16s(fin_ga, go, sw, 0);
      asm volatile("buffer_load_dword %0, %1, %2, 0 offen sc1" : "=v"(fin_om) : "v"(O_OMG + fs * 4), "s"(sw) : "memory");
      asm volatile("s_waitcnt vmcnt(0)" : "+v"(fin_ga), "+v"(fin_om)::"memory");
    }
    STAMP(12);
  }
  WAITL();
  __builtin_amdgcn_s_barrier();
  asm volatile("" ::: "memory");
  {
    const int sl = tid >> 3;
    const u32 ro = L_G + sl * 272 + (tid & 7) * 32;
    f4v s0v = *(LAS f4v*)(L + ro), s1v = *(LAS f4v*)(L + ro + 16);
#pragma unroll
    for (int l = 1; l < 4; ++l) { s0v += *(LAS f4v*)(L + ro + l * 32768); s1v += *(LAS f4v*)(L + ro + l * 32768 + 16); }
    const float b2 = blo(fin_b2), om = __uint_as_float(fin_om);
    v4u o;
#pragma unroll
    for (int i = 0; i < 4; ++i) {
      const float e0 = i < 2 ? s0v[2 * i] : s1v[2 * i - 4], e1 = i < 2 ? s0v[2 * i + 1] : s1v[2 * i - 3];
      const u32 y = pk(e0, e1);
      const u32 yb = pk(blo(y) + b2, bhi(y) + b2);
      const u32 m = pk(om * blo(yb), om * bhi(yb));
      o[i] = pk(blo(fin_ga[i]) + blo(m), bhi(fin_ga[i]) + bhi(m));
    }
    *(v4u*)(p.out + (size_t)(64 * st + sl) * 512 + dt * 64 + (tid & 7) * 8) = o;
  }
}

__global__ void pre_kernel(u64* d) {
  u64 t0, t1, pp; const auto ka = __builtin_amdgcn_kernarg_segment_ptr();
  asm volatile("s_memrealtime %0\n\ts_waitcnt lgkmcnt(0)\n\ts_load_dwordx2 %2, %3, 0x0\n\ts_waitcnt lgkmcnt(0)\n\ts_memrealtime %1\n\ts_waitcnt lgkmcnt(0)" : "=&s"(t0), "=&s"(t1), "=&s"(pp) : "s"(ka));
  if (threadIdx.x == 0 && blockIdx.x == 0) ((u64*)pp)[0] = t1 - t0;
}
__global__ void amp_kernel(u64* sink) {
  if (threadIdx.x == 0) { u64 dur = sink[8] * AMPM; if (dur > 500000) dur = 500000; const u64 t0 = __builtin_amdgcn_s_memrealtime(); while (__builtin_amdgcn_s_memrealtime() - t0 < dur) __builtin_amdgcn_s_sleep(8); sink[0] = dur; }
}
extern "C" void kernel_launch(void* const* d_in, const int* in_sizes, int n_in, void* d_out, int out_size, void* d_ws, size_t ws_size,
                              hipStream_t stream) {
  Params p;
  p.x = (const u16*)d_in[0]; p.wr = (const u16*)d_in[1]; p.wi = (const u16*)d_in[2];
  p.w1 = (const u16*)d_in[4]; p.b1 = (const u16*)d_in[5]; p.w2 = (const u16*)d_in[6]; p.b2 = (const u16*)d_in[7]; p.gw = (const u16*)d_in[8];
  p.out = (u16*)d_out;
  p.ws = (char*)d_ws + ((ws_size - WS_BYTES) & ~(size_t)0xFFFFF);
  (void)hipFuncSetAttribute((const void*)fused_kernel, hipFuncAttributeMaxDynamicSharedMemorySize, LDS_BYTES);
  pre_kernel<<<1,64,0,stream>>>((u64*)(p.ws + O_DIAG + 600000) + 8);
  fused_kernel<<<256, 512, LDS_BYTES, stream>>>(p);
  amp_kernel<<<1, 64, 0, stream>>>((u64*)(p.ws + O_DIAG + 600000));
}
```

```cpp
#include <hip/hip_runtime.h>
#include <stdint.h>

typedef unsigned u32;
typedef unsigned long long u64;
typedef unsigned short u16;
typedef u32 v4u __attribute__((ext_vector_type(4)));
typedef u32 v2u __attribute__((ext_vector_type(2)));
typedef int v4i __attribute__((ext_vector_type(4)));
typedef short s4v __attribute__((ext_vector_type(4)));
typedef short s8v __attribute__((ext_vector_type(8)));
typedef float f2v __attribute__((ext_vector_type(2)));
typedef float f4v __attribute__((ext_vector_type(4)));
typedef float f16v __attribute__((ext_vector_type(16)));
typedef __bf16 b2v __attribute__((ext_vector_type(2)));
#define LAS __attribute__((address_space(3)))
#define DI __device__ __forceinline__
typedef __amdgpu_buffer_rsrc_t rsrc_t;

constexpr u32 O_EPOCH = 0, O_F1X = 106496  , O_F2 = 32768, O_F1H = 147456, O_XN = 65536  , O_VN = 98304, O_DIAG = 262144;
constexpr u32 O_H = 1u << 20, O_X1 = 3u << 20, O_V = 5u << 20, O_GA = 7u << 20, O_U = 9u << 20, WS_BYTES = 11u << 20;
constexpr u32 POISON = 0xFFFFFFFFu;
constexpr int AUX_SC1 = 16, AUX_NT = 2;
constexpr u32 SPIN_CAP = 100000;
constexpr u32 L_G = 0;
constexpr u32 L_S = 131072;
constexpr u32 L_X = L_S, L_HR = L_S + 8704, L_T = L_S + 17408;
constexpr u32 L_E = L_S, L_W = L_S + 8704, L_O = L_S + 17408;
constexpr u32 L_U = L_S;
constexpr u32 L_GA = L_S + 17408;
constexpr u32 L_C = L_S + 26624;
constexpr u32 L_B = L_C + 256, L_D = L_C + 1536;
constexpr u32 LDS_BYTES = 163840;
constexpr u32 C_H = L_C, C_GW = L_C + 16, C_TAG = L_C + 32, C_OK = L_C + 48, C_G = L_C + 64, C_NY = L_C + 96;

struct Params {
  const u16 *x, *wr, *wi, *w1, *b1, *w2, *b2, *gw;
  u16* out;
  char* ws;
};

#define THR 32
#define DMAX 8
#define NTB 1
#define NTW2 4
#define W2AT 23
#define W2NMAX 4
#define GAAT 12
#define GOAT 47
#define SPD 24
#define SPN 0
#define WGAT 1
#define FFT 3
#define HELP 1
#define OWNWAIT 0
#define SPPH 2
#define SPNT 0
#define SPW 1
#define SPGO 0
#define OWNTHR 8
#define F1XS 128
#define POLLAT 44
#define OWNA 4
#define XDLY 0
#define HACK 0
#define SPD2 32
#define CONSTA 1
#define F1HS 128
#define W2PRE 0
#define VSLEEP 8

DI u32 pk(float a, float b) { f2v v = {a, b}; return __builtin_bit_cast(u32, __builtin_convertvector(v, b2v)); }
DI u32 pk2(f2v v) { return __builtin_bit_cast(u32, __builtin_convertvector(v, b2v)); }
DI float blo(u32 v) { return __uint_as_float(v << 16); }
DI float bhi(u32 v) { return __uint_as_float(v & 0xFFFF0000u); }
DI f2v up(u32 v) { const f2v r = {blo(v), bhi(v)}; return r; }
DI float rbf(float a) { return blo(pk(a, a)); }
DI u32 san(u32 v) { return v < 0xFFFFFFFEu ? v : 0xFFFFFFFEu; }
DI u32 max4(v4u v) { return max(max(v.x, v.y), max(v.z, v.w)); }
DI f4v mfma16(v4u a, v4u b, f4v c) { return __builtin_amdgcn_mfma_f32_16x16x32_bf16(__builtin_bit_cast(s8v, a), __builtin_bit_cast(s8v, b), c, 0, 0, 0); }
DI f16v mfma32(v4u a, v4u b, f16v c) { return __builtin_amdgcn_mfma_f32_32x32x16_bf16(__builtin_bit_cast(s8v, a), __builtin_bit_cast(s8v, b), c, 0, 0, 0); }
DI v2u trd(LAS char* L, u32 off) { return __builtin_bit_cast(v2u, __builtin_amdgcn_ds_read_tr16_b64_v4i16((LAS s4v*)(L + off))); }
DI v4u trd2(LAS char* L, u32 o0, u32 o1) { v2u a = trd(L, o0), b = trd(L, o1); v4u r = {a.x, a.y, b.x, b.y}; return r; }
template <int C> DI float dppadd(float v) { return v + __int_as_float(__builtin_amdgcn_update_dpp(0, __float_as_int(v), C, 0xF, 0xF, true)); }
DI float dsum16(float v) { v = dppadd<0xB1>(v); v = dppadd<0x4E>(v); v = dppadd<0x141>(v); return dppadd<0x140>(v); }
DI float sw16(float a, float b) { auto r = __builtin_amdgcn_permlane16_swap(__float_as_uint(a), __float_as_uint(b), false, false); return __uint_as_float(r[0]) + __uint_as_float(r[1]); }
DI float sw32(float a, float b) { auto r = __builtin_amdgcn_permlane32_swap(__float_as_uint(a), __float_as_uint(b), false, false); return __uint_as_float(r[0]) + __uint_as_float(r[1]); }
DI float rdl(float v, int l) { return __int_as_float(__builtin_amdgcn_readlane(__float_as_int(v), l)); }
DI u64 mix64(u64 z) { z += 0x9E3779B97F4A7C15ull; z = (z ^ (z >> 30)) * 0xBF58476D1CE4E5B9ull; z = (z ^ (z >> 27)) * 0x94D049BB133111EBull; return z ^ (z >> 31); }
#define WAITL() asm volatile("s_waitcnt lgkmcnt(0)" ::: "memory")
template <int N> DI void waitv() { asm volatile("s_waitcnt vmcnt(%0)" ::"n"(N) : "memory"); }
#define SCHED() __builtin_amdgcn_sched_barrier(0)
#define FENCE() asm volatile("" ::: "memory")
#define PIN(x) asm volatile("" : "+v"(x))
#define SPIN(var) _Pragma("unroll 1") for (u32 var = 0; var < SPIN_CAP; ++var)
#define FCOS __builtin_amdgcn_cosf
#define FSIN __builtin_amdgcn_sinf
DI void lsig(LAS char* L, u32 slot, int w, u32 ph) { WAITL(); *(volatile LAS u32*)(L + slot + w * 4) = ph; }
template <int SL = 0> DI void lwait(LAS char* L, u32 slot, u32 ph) {
  _Pragma("unroll 1") for (u32 s = 0; s < 40u * SPIN_CAP; ++s) {
    FENCE();
    const v4u t = *(LAS v4u*)(L + slot);
    if ((u32)__builtin_amdgcn_readfirstlane(min(min(t.x, t.y), min(t.z, t.w))) >= ph) break;
    if (SL) __builtin_amdgcn_s_sleep(SL);
  }
  FENCE();
}
DI void lwait1(LAS char* L, u32 a, u32 ph) {
  _Pragma("unroll 1") for (u32 s = 0; s < 40u * SPIN_CAP && (u32)__builtin_amdgcn_readfirstlane(*(volatile LAS u32*)(L + a)) < ph; ++s) __builtin_amdgcn_s_sleep(1);
  FENCE();
}
DI void lsync(LAS char* L, u32 slot, int w, u32 ph) { lsig(L, slot, w, ph); lwait(L, slot, ph); }
DI void wgbar() { WAITL(); __builtin_amdgcn_s_barrier(); FENCE(); }

#define STAMP(i)

template <int SGN> DI f2v addi(f2v a, f2v b) { const f2v t = {b.y, b.x}, s = {-(float)SGN, (float)SGN}; return __builtin_elementwise_fma(t, s, a); }
template <int SGN> DI void dft8(const f2v (&a)[8], f2v (&y)[8]) {
  const float R = 0.70710678118654752f;
  const f2v b0 = a[0] + a[4], b4 = a[0] - a[4], b1 = a[1] + a[5], d1 = a[1] - a[5], b2 = a[2] + a[6], d2 = a[2] - a[6], b3 = a[3] + a[7], d3 = a[3] - a[7];
  const f2v b5 = addi<SGN>(d1, d1) * R, b7 = addi<-SGN>(d3, d3) * (-R);
  const f2v e = b0 + b2, f = b0 - b2, g = b1 + b3, h = b1 - b3;
  y[0] = e + g; y[4] = e - g; y[2] = addi<SGN>(f, h); y[6] = addi<-SGN>(f, h);
  const f2v e2 = addi<SGN>(b4, d2), f2 = addi<-SGN>(b4, d2), g2 = b5 + b7, h2 = b5 - b7;
  y[1] = e2 + g2; y[5] = e2 - g2; y[3] = addi<SGN>(f2, h2); y[7] = addi<-SGN>(f2, h2);
}
DI v4u cmulpk(f4v r, f4v m, f4v C, f4v S) {
  const f4v a = r * C - m * S, b = m * C + r * S;
  const v4u t = {pk(a[0], b[0]), pk(a[1], b[1]), pk(a[2], b[2]), pk(a[3], b[3])};
  return t;
}
struct P2C { v4u f1r, f1i, fr, fi, gr, gi, g1r, g1i; f4v tc, ts, uc, us; };
DI void p2_pair(const P2C& c, v4u& a, v4u& b, v4u wa, v4u wb) {
  const f4v z = {0.f, 0.f, 0.f, 0.f};
  f4v r0 = mfma16(a, c.f1r, z), m0 = mfma16(a, c.f1i, z), r1 = mfma16(b, c.f1r, z), m1 = mfma16(b, c.f1i, z);
  const f4v ca = {blo(wa[0]), blo(wa[1]), blo(wa[2]), blo(wa[3])}, sa = {bhi(wa[0]), bhi(wa[1]), bhi(wa[2]), bhi(wa[3])};
  SCHED();
  a = cmulpk(r0, m0, c.tc, c.ts); r0 = mfma16(c.fr, a, z); m0 = mfma16(c.fi, a, z);
  SCHED();
  b = cmulpk(r1, m1, c.tc, c.ts); r1 = mfma16(c.fr, b, z); m1 = mfma16(c.fi, b, z);
  SCHED();
  const f4v cb = {blo(wb[0]), blo(wb[1]), blo(wb[2]), blo(wb[3])}, sb = {bhi(wb[0]), bhi(wb[1]), bhi(wb[2]), bhi(wb[3])};
  a = cmulpk(r0, m0, ca, sa); r0 = mfma16(a, c.gr, z); m0 = mfma16(a, c.gi, z);
  SCHED();
  b = cmulpk(r1, m1, cb, sb); r1 = mfma16(b, c.gr, z); m1 = mfma16(b, c.gi, z);
  SCHED();
  a = cmulpk(r0, m0, c.uc, c.us); r0 = mfma16(c.g1r, a, z); m0 = mfma16(c.g1i, a, z);
  SCHED();
  b = cmulpk(r1, m1, c.uc, c.us); r1 = mfma16(c.g1r, b, z); m1 = mfma16(c.g1i, b, z);
  SCHED();
#pragma unroll
  for (int i = 0; i < 4; ++i) a[i] = san(pk(r0[i], m0[i]));
#pragma unroll
  for (int i = 0; i < 4; ++i) b[i] = san(pk(r1[i], m1[i]));
}

template <bool NT> DI void dma16(u32 ldsaddr, u32 voff, v4i srd, u32 soff) {
  if constexpr (NT) asm volatile("s_mov_b32 m0, %0\n\ts_nop 0\n\tbuffer_load_dwordx4 %1, %2, %3 offen nt lds" ::"s"(ldsaddr), "v"(voff), "s"(srd), "s"(soff) : "memory");
  else asm volatile("s_mov_b32 m0, %0\n\ts_nop 0\n\tbuffer_load_dwordx4 %1, %2, %3 offen lds" ::"s"(ldsaddr), "v"(voff), "s"(srd), "s"(soff) : "memory");
}
DI void dma4(u32 ldsaddr, u32 voff, v4i srd, u32 soff) {
  asm volatile("s_mov_b32 m0, %0\n\ts_nop 0\n\tbuffer_load_dword %1, %2, %3 offen lds" ::"s"(ldsaddr), "v"(voff), "s"(srd), "s"(soff) : "memory");
}
DI void dma4nt(u32 ldsaddr, u32 voff, v4i srd, u32 soff) {
  asm volatile("s_mov_b32 m0, %0\n\ts_nop 0\n\tbuffer_load_dword %1, %2, %3 offen nt lds" ::"s"(ldsaddr), "v"(voff), "s"(srd), "s"(soff) : "memory");
}
DI void ld16o(int imm, v4u& d, u32 voff, v4i srd, u32 soff) { asm volatile("buffer_load_dwordx4 %0, %1, %2, %3 offen offset:%4 sc1" : "=v"(d) : "v"(voff), "s"(srd), "s"(soff), "n"(imm) : "memory"); }
DI void ld8s(v2u& d, u32 voff, v4i srd) { asm volatile("buffer_load_dwordx2 %0, %1, %2, 0 offen sc1" : "=v"(d) : "v"(voff), "s"(srd) : "memory"); }
DI v2u ld8w(u32 voff, v4i srd) { v2u d; asm volatile("buffer_load_dwordx2 %0, %1, %2, 0 offen sc1\n\ts_waitcnt vmcnt(0)" : "=&v"(d) : "v"(voff), "s"(srd) : "memory"); return d; }
DI void st16s(v4u d, u32 voff, v4i srd, u32 soff) { asm volatile("buffer_store_dwordx4 %0, %1, %2, %3 offen sc1\n\ts_nop 1" ::"v"(d), "v"(voff), "s"(srd), "s"(soff) : "memory"); }
DI void st8s(v2u d, u32 voff, v4i srd) { asm volatile("buffer_store_dwordx2 %0, %1, %2, 0 offen sc1\n\ts_nop 1" ::"v"(d), "v"(voff), "s"(srd) : "memory"); }
template <int N> DI void waitv2(v4u& a, v4u& b) { asm volatile("s_waitcnt vmcnt(%2)" : "+v"(a), "+v"(b) : "n"(N) : "memory"); }
DI v4i mksrd(const void* p, u32 bytes) { const u64 a = (u64)p; v4i r = {(int)(u32)a, (int)((u32)(a >> 32) & 0xFFFFu), (int)bytes, 0x00020000}; return r; }
template <int SL> DI void pollflags(u32 off, v4i sw, v2u tg) {
  SPIN(s) {
    const v2u f = ld8w(off, sw);
    if (f.x == tg.x && f.y == tg.y) off = WS_BYTES;
    if (__all(off == WS_BYTES)) break;
    __builtin_amdgcn_s_sleep(SL);
  }
}

constexpr int R0 = 6;
constexpr int imin(int a, int b) { return a < b ? a : b; }
constexpr int issued(int k, int n) { return imin(n, imin(R0 + 2 * (k + 1), k + 1 + DMAX)); }
constexpr int W2N = imin(32 - W2AT, W2NMAX);
constexpr int g1_extra(int k) { return (k == 15 ? 8 : 0) + (k >= W2AT && k < W2AT + W2N ? 2 : 0); }
template <int G> constexpr int ops_after(int k) {
  const int n = G == 1 ? 32 : 28;
  int c = 0; bool seen = false;
  for (int j = -1; j < k; ++j) {
    for (int s = (j < 0 ? 0 : issued(j - 1, n)); s < issued(j, n); ++s) { if (seen) c += 2; if (s == k) seen = true; }
    if (seen && j >= 0 && G == 1) c += g1_extra(j);
  }
  return c;
}
template <int K> struct IC { static constexpr int v = K; };
template <int B, int E, class Fn> DI void sfor(Fn&& f) { if constexpr (B < E) { f(IC<B>{}); sfor<B + 1, E>(f); } }

#define INDICES() \
  int wv = wv_, b = b_, tid = tid_; \
  asm volatile("" : "+s"(wv), "+s"(b), "+v"(tid)); \
  const int lane = tid & 63, w = wv & 3, xl = b & 7, idx = b >> 3, dt = idx & 7, st = 4 * xl + (idx >> 3), s0 = 32 * xl + idx, q = lane >> 4, c = lane & 15; \
  (void)dt; (void)st; (void)s0; (void)q; (void)c; (void)w; (void)xl;

__global__ void __launch_bounds__(512, 2) fused_kernel(Params p) {
  extern __shared__ __attribute__((aligned(16))) char smem[];
  LAS char* L = (LAS char*)smem;
  const int tid_ = threadIdx.x, wv_ = __builtin_amdgcn_readfirstlane(tid_ >> 6), b_ = blockIdx.x;
  const bool spare = b_ >= 224;
  STAMP(0);
  asm volatile("" ::"s"(p.x), "s"(p.wr), "s"(p.wi), "s"(p.w1), "s"(p.b1), "s"(p.w2), "s"(p.b2), "s"(p.gw), "s"(p.out), "s"(p.ws));
  const rsrc_t rw = __builtin_amdgcn_make_buffer_rsrc((void*)p.ws, 0, (int)WS_BYTES, 0x00020000);
  const v4i sw = mksrd(p.ws, WS_BYTES);
  const u32 lbase = (u32)(size_t)smem;

  v4u fin_ga; u32 fin_om;
  if (wv_ < 4) {
    INDICES();
    __builtin_amdgcn_s_setprio(3);
    const rsrc_t rx = __builtin_amdgcn_make_buffer_rsrc((void*)p.x, 0, 2048 * 512 * 2, 0x00020000);
    const v4u x0 = __builtin_amdgcn_raw_buffer_load_b128(rx, lane * 16, (s0 + 512 * w) * 1024, AUX_NT);
    const v4u x1 = __builtin_amdgcn_raw_buffer_load_b128(rx, lane * 16, (s0 + 512 * w + 256) * 1024, AUX_NT);
    const v2u epv = __builtin_amdgcn_raw_buffer_load_b64(rw, O_EPOCH, 0, 0);
    const v4u gwv = *(const v4u*)(p.gw + lane * 8);
    if (tid < 16) ((LAS u32*)(L + L_C))[tid] = 0;
    wgbar();
    const int j1 = xl, kb = idx;
    if (FFT >= 2 && w >= 2) {
      const u32 o = ((u32)(j1 + 8 * (8 * kb + (lane >> 3))) * 514u + min((lane & 7) * 128, 512)) & ~3u;
      dma4nt(lbase + L_D, o, mksrd(w == 2 ? p.wr : p.wi, 2048 * 257 * 2), 0);
    }
    v4u f64r[2], f64i[2], a8;
    f4v twc, tws;
    auto constA = [&]() {
      const float k1 = (float)(16 * w + c), kq = k1 * (1.f / 64), a0 = (float)(8 * q) * kq;
#pragma unroll
      for (int ks = 0; ks < 2; ++ks)
#pragma unroll
        for (int j = 0; j < 4; ++j) {
          const float t0 = a0 + (float)(32 * ks + 2 * j) * kq, t1 = t0 + kq;
          f64r[ks][j] = pk(FCOS(t0), FCOS(t1));
          f64i[ks][j] = pk(FSIN(-t0), FSIN(-t1));
        }
      PIN(f64r[0]); PIN(f64r[1]); PIN(f64i[0]); PIN(f64i[1]);
    };
    if (CONSTA == 0) constA();
    f2v xa[4], xb[4];
#pragma unroll
    for (int i = 0; i < 4; ++i) { xa[i] = up(x0[i]); xb[i] = up(x1[i]); }
    STAMP(1);
    *(LAS v4u*)(L + L_X + (2 * w) * 1088 + lane * 16) = x0;
    *(LAS v4u*)(L + L_X + (2 * w + 1) * 1088 + lane * 16) = x1;
    {
      const float pa = rdl(xa[0].x, 0), pb = rdl(xb[0].x, 0);
      f2v da[4], db[4];
#pragma unroll
      for (int i = 0; i < 4; ++i) { da[i] = xa[i] - pa; db[i] = xb[i] - pb; }
      const f2v s1a = (da[0] + da[1]) + (da[2] + da[3]), s1b = (db[0] + db[1]) + (db[2] + db[3]);
      const f2v s2a = da[0] * da[0] + da[1] * da[1] + da[2] * da[2] + da[3] * da[3], s2b = db[0] * db[0] + db[1] * db[1] + db[2] * db[2] + db[3] * db[3];
      const float t = sw32(sw16(dsum16(s1a.x + s1a.y), dsum16(s1b.x + s1b.y)), sw16(dsum16(s2a.x + s2a.y), dsum16(s2b.x + s2b.y)));
      const f2v m1 = f2v{rdl(t, 0), rdl(t, 16)} * (1.f / 512.f), m2 = f2v{rdl(t, 32), rdl(t, 48)} * (1.f / 512.f);
      const f2v var = __builtin_elementwise_max(m2 - m1 * m1, f2v{0.f, 0.f});
      const f2v ve = up(pk2(up(pk2(var)) + 1e-5f));
      const f2v sd = up(pk(__builtin_amdgcn_sqrtf(ve.x), __builtin_amdgcn_sqrtf(ve.y)));
      const f2v rs = {__builtin_amdgcn_rcpf(sd.x), __builtin_amdgcn_rcpf(sd.y)};
      const f2v nm = -up(pk2(m1 + f2v{pa, pb})) * rs;
      v4u ha, hb;
#pragma unroll
      for (int i = 0; i < 4; ++i) { ha[i] = pk2(xa[i] * rs.x + nm.x); hb[i] = pk2(xb[i] * rs.y + nm.y); }
      *(LAS v4u*)(L + L_HR + (2 * w) * 1088 + lane * 16) = ha;
      *(LAS v4u*)(L + L_HR + (2 * w + 1) * 1088 + lane * 16) = hb;
    }
    const u64 tag = mix64(((u64)(u32)__builtin_amdgcn_readfirstlane(epv.y) << 32) | (u32)__builtin_amdgcn_readfirstlane(epv.x));
    const v2u tg = {(u32)tag, (u32)(tag >> 32)};
    *(LAS v2u*)(L + C_TAG) = tg;
    lsync(L, C_H, w, 1);
    STAMP(11);
    {
      const u32 ta = L_HR + (c >> 2) * 1088 + (128 * w + 16 * q + 4 * (c & 3)) * 2;
      const v4u h0 = trd2(L, ta, ta + 4 * 1088), h1 = trd2(L, ta + 128, ta + 128 + 4 * 1088);
      const u32 vo = (lane >> 5) * 131072 + (lane & 31) * 16;
      __builtin_amdgcn_raw_buffer_store_b128(h0, rw, vo, O_H + (4 * w) * 131072 + s0 * 512, AUX_SC1);
      __builtin_amdgcn_raw_buffer_store_b128(h1, rw, vo, O_H + (4 * w + 2) * 131072 + s0 * 512, AUX_SC1);
    }
    const rsrc_t rr = __builtin_amdgcn_make_buffer_rsrc((void*)p.wr, 0, 2048 * 257 * 2, 0x00020000);
    const rsrc_t ri = __builtin_amdgcn_make_buffer_rsrc((void*)p.wi, 0, 2048 * 257 * 2, 0x00020000);
    const u32 wof = (u32)(j1 + 8 * (64 * w + lane)) * 514u + kb * 16;
    v4u wq0, wq1, wn = {0u, 0u, 0u, 0u};
    const bool nyq = kb == 31 && w == 3;
    auto wgather = [&]() {
      wq0 = __builtin_amdgcn_raw_buffer_load_b128(rr, wof, 0, 0); wq1 = __builtin_amdgcn_raw_buffer_load_b128(ri, wof, 0, 0);
      if (nyq) {
#pragma unroll
        for (int i = 0; i < 4; ++i) {
          const u32 o2 = (u32)(j1 + 8 * (64 * q + 16 * i + c)) * 514u + 512u;
          wn[i] = (u32)__builtin_amdgcn_raw_buffer_load_b16(rr, o2, 0, 0) | ((u32)__builtin_amdgcn_raw_buffer_load_b16(ri, o2, 0, 0) << 16);
        }
      }
    };
    auto wimage = [&]() {
      const u32 epos = ((w * 16 + c) * 4 + q) * 4;
#pragma unroll
      for (int i = 0; i < 4; ++i) {
        u32 lo = __builtin_amdgcn_perm(wq1[i], wq0[i], 0x05040100u), hi = __builtin_amdgcn_perm(wq1[i], wq0[i], 0x07060302u);
        if (i == 0 && kb == 0) lo = pk2(up(lo) * 0.5f);
        *(LAS u32*)(L + L_W + (2 * i) * 1024 + epos) = lo;
        *(LAS u32*)(L + L_W + (2 * i + 1) * 1024 + epos) = hi;
      }
    };
    FENCE();
    *(volatile LAS u32*)(L + C_H + w * 4) = 2u;
    STAMP(2);
    if (!spare) lwait(L, C_GW, 1);
    if (FFT >= 2) {
      const v4u P = {POISON, POISON, POISON, POISON};
      __builtin_amdgcn_raw_buffer_store_b128(P, rw, lane * 16, O_V + j1 * 262144 + kb * 8192 + w * 2048, AUX_SC1);
      __builtin_amdgcn_raw_buffer_store_b128(P, rw, lane * 16, O_V + j1 * 262144 + kb * 8192 + w * 2048 + 1024, AUX_SC1);
      __builtin_amdgcn_raw_buffer_store_b128(P, rw, lane * 16, O_GA + b * 8192 + w * 2048, AUX_SC1);
      __builtin_amdgcn_raw_buffer_store_b128(P, rw, lane * 16, O_GA + b * 8192 + w * 2048 + 1024, AUX_SC1);
      __builtin_amdgcn_raw_buffer_store_b128(P, rw, nyq ? O_VN + j1 * 1024 + lane * 16 : WS_BYTES, 0, AUX_SC1);
    }
    FENCE();
    if (CONSTA == 1) constA();
    {
      const float k1 = (float)(16 * w + c), b0 = (float)(4 * (q & 1)), k2 = (float)(c >> 2) * 0.125f, ph = (c & 1) ? 0.25f : 0.f, kt = k1 * (-1.f / 512);
      const bool on = ((c >> 1) & 1) == (q >> 1);
#pragma unroll
      for (int r = 0; r < 4; ++r) {
        const float bb = b0 + (float)r, t = bb * k2 + ph;
        const u32 e = pk(FCOS(t), FSIN(t));
        a8[r] = on ? e : 0u;
        twc[r] = FCOS(bb * kt); tws[r] = FSIN(bb * kt);
      }
    }
    if (HACK == 0) {
      PIN(a8); PIN(twc); PIN(tws);
      waitv<(FFT >= 2 ? 5 : 0)>();
      lsig(L, C_H, w, 3);
      if (w == 1) { lwait(L, C_H, 3); if (lane == 0) __builtin_amdgcn_raw_buffer_store_b64(tg, rw, O_F1H + s0 * F1HS, 0, AUX_SC1); }
      STAMP(3);
    }
    v4u o0, o1;
    f2v ny[8];
    if (FFT >= 1) {
      f2v y[8], zz[8];
      const f4v z = {0.f, 0.f, 0.f, 0.f};
      const u32 xo = L_X + ((c >> 1) & 1) * 1088 + (8 * q + (c >> 2)) * 16 + 8 * (c & 1);
      v4u a8n = a8; a8n[1] ^= 0x80008000u; a8n[3] ^= 0x80008000u;
#pragma unroll
      for (int pi = 0; pi < 4; ++pi) {
        const v4u A0 = trd2(L, xo + pi * 2176, xo + pi * 2176 + 64), A1 = trd2(L, xo + pi * 2176 + 512, xo + pi * 2176 + 576);
        const v4u t = cmulpk(mfma16(A1, f64r[1], mfma16(A0, f64r[0], z)), mfma16(A1, f64i[1], mfma16(A0, f64i[0], z)), twc, tws);
        const f4v d = mfma16(a8, t, z), e = mfma16(a8n, t, z);
        y[2 * pi] = f2v{d[0], d[1]}; y[2 * pi + 1] = f2v{d[2], d[3]};
        ny[2 * pi] = f2v{e[0], e[1]}; ny[2 * pi + 1] = f2v{e[2], e[3]};
      }
      dft8<-1>(y, zz);
#pragma unroll
      for (int j = 0; j < 8; ++j) *(LAS u32*)(L + L_T + w * 2048 + j * 256 + lane * 4) = pk2(zz[j]);
      o0 = *(LAS v4u*)(L + L_T + w * 2048 + lane * 16); o1 = *(LAS v4u*)(L + L_T + w * 2048 + 1024 + lane * 16);
      if (HACK == 1) { PIN(o0); PIN(o1); }
    }
    if (HACK == 1) {
      waitv<(FFT >= 2 ? 5 : 0)>();
      lsig(L, C_H, w, 3);
      if (w == 1) { lwait(L, C_H, 3); if (lane == 0) __builtin_amdgcn_raw_buffer_store_b64(tg, rw, O_F1H + s0 * F1HS, 0, AUX_SC1); }
      STAMP(3);
    }
    if (FFT >= 1) {
      f2v zz[8];
      const u32 vo = q * 262144 + (((lane >> 2) & 3) * 64 + (lane & 3) * 4) * 4;
      __builtin_amdgcn_raw_buffer_store_b128(o0, rw, vo, O_X1 + s0 * 1024 + w * 64, AUX_SC1);
      __builtin_amdgcn_raw_buffer_store_b128(o1, rw, vo, O_X1 + 4 * 262144 + s0 * 1024 + w * 64, AUX_SC1);
      if (w == 0) {
        dft8<-1>(ny, zz);
        if (lane == 0) {
          const v4u n0 = {pk2(zz[0]), pk2(zz[1]), pk2(zz[2]), pk2(zz[3])}, n1 = {pk2(zz[4]), pk2(zz[5]), pk2(zz[6]), pk2(zz[7])};
          __builtin_amdgcn_raw_buffer_store_b128(n0, rw, O_XN + s0 * 128, 0, AUX_SC1);
          __builtin_amdgcn_raw_buffer_store_b128(n1, rw, O_XN + s0 * 128 + 16, 0, AUX_SC1);
        }
      }
    }
    if (FFT >= 2 && WGAT == 0) wgather();
    FENCE();
    STAMP(12);
    {
      float ga = 0.f, gb = 0.f;
      const b2v one = {(__bf16)1.f, (__bf16)1.f};
#pragma unroll
      for (int i = 0; i < 4; ++i) {
        const f2v g = up(gwv[i]);
        ga = __builtin_amdgcn_fdot2_f32_bf16(__builtin_convertvector(xa[i] * g, b2v), one, ga, false);
        gb = __builtin_amdgcn_fdot2_f32_bf16(__builtin_convertvector(xb[i] * g, b2v), one, gb, false);
      }
      float t = sw16(dsum16(ga), dsum16(gb));
      t = rbf(sw32(t, t));
      const float g = rbf(__builtin_amdgcn_rcpf(1.f + __builtin_amdgcn_exp2f(-1.44269504088896f * t)));
      if ((lane & 47) == 0) {
        *(LAS float*)(L + C_G + (2 * w + q) * 4) = g;
        __builtin_amdgcn_raw_buffer_store_b32(__float_as_uint(rbf(1.f - g)), rw, O_XN + s0 * 128 + 32 + (2 * w + q) * 4, 0, AUX_SC1);
      }
    }
    if (FFT >= 2 && WGAT == 1) wgather();
    P2C pc;
    {
      const float cf = (float)c, jf = (float)j1, pj = (8.f * cf + jf) * (1.f / 128), e0 = (float)(4 * q);
#pragma unroll
      for (int i = 0; i < 4; ++i) {
        const float e = e0 + (float)i;
        { const float t = e * pj, cs = FCOS(t), sn = FSIN(t); pc.f1r[i] = pk(cs, sn); pc.f1i[i] = pk(-sn, cs); pc.tc[i] = FCOS(t * -0.0625f); pc.ts[i] = FSIN(t * -0.0625f); }
        { const float t = e * cf * 0.0625f, cs = FCOS(t), sn = FSIN(t); pc.fr[i] = pk(cs, sn); pc.fi[i] = pk(-sn, cs); pc.gr[i] = pk(cs, -sn); pc.gi[i] = pk(sn, cs); }
        { const float t = cf * (8.f * e + jf) * (1.f / 128), cs = FCOS(t), sn = FSIN(t); pc.g1r[i] = pk(cs, -sn); pc.g1i[i] = pk(sn, cs); pc.uc[i] = FCOS(t * 0.0625f); pc.us[i] = FSIN(t * 0.0625f); }
      }
      PIN(pc.f1r); PIN(pc.f1i); PIN(pc.fr); PIN(pc.fi); PIN(pc.gr); PIN(pc.gi); PIN(pc.g1r); PIN(pc.g1i); PIN(pc.tc); PIN(pc.ts); PIN(pc.uc); PIN(pc.us);
    }
    waitv<0>();
    STAMP(4);
    lsync(L, C_H, w, 4);
    if (w == 1 && lane == 0) __builtin_amdgcn_raw_buffer_store_b64(tg, rw, O_F1X + s0 * F1XS, 0, AUX_SC1);
    if (FFT >= 2 && WGAT < 2) wimage();
    lwait1(L, C_OK + w * 4, 1);
    if (XDLY) __builtin_amdgcn_s_sleep(XDLY);
    pollflags<8>(O_F1X + (64 * w + lane) * F1XS, sw, tg);
    STAMP(5);
    if (FFT >= 2) {
      if (WGAT == 2) wgather();
      const int sp = 64 * w + (lane >> 1);
      const u32 xo = O_X1 + j1 * 262144 + sp * 1024 + kb * 32 + (lane & 1) * 16;
      v4u e0 = __builtin_amdgcn_raw_buffer_load_b128(rw, xo, 0, AUX_SC1), e1 = __builtin_amdgcn_raw_buffer_load_b128(rw, xo + 32768, 0, AUX_SC1);
      if (WGAT == 2) wimage();
      const u32 ep = (lane & 1) * 4096 + ((w * 16 + (sp & 15)) * 4 + ((sp >> 4) & 1)) * 4;
#pragma unroll
      for (int i = 0; i < 4; ++i) { *(LAS u32*)(L + L_E + i * 1024 + ep) = e0[i]; *(LAS u32*)(L + L_E + i * 1024 + ep + 8) = e1[i]; }
      STAMP(6);
      lsync(L, C_H, w, 5);
      v4u in = {0u, 0u, 0u, 0u};
      if (nyq) {
#pragma unroll
        for (int i = 0; i < 4; ++i) in[i] = __builtin_amdgcn_raw_buffer_load_b32(rw, O_XN + (64 * q + 16 * i + c) * 128 + j1 * 4, 0, AUX_SC1);
      }
      if (b == 0 && tid == 0) *(u64*)(p.ws + O_EPOCH) = tag;
      v4u ca = *(LAS v4u*)(L + L_E + (2 * w) * 1024 + lane * 16), cb = *(LAS v4u*)(L + L_E + (2 * w + 1) * 1024 + lane * 16);
      p2_pair(pc, ca, cb, *(LAS v4u*)(L + L_W + (2 * w) * 1024 + lane * 16), *(LAS v4u*)(L + L_W + (2 * w + 1) * 1024 + lane * 16));
      *(LAS v4u*)(L + L_O + (2 * w) * 1024 + lane * 16) = ca;
      *(LAS v4u*)(L + L_O + (2 * w + 1) * 1024 + lane * 16) = cb;
      lsync(L, C_H, w, 6);
#pragma unroll
      for (int i = 0; i < 4; ++i) { e0[i] = *(LAS u32*)(L + L_O + i * 1024 + ep); e1[i] = *(LAS u32*)(L + L_O + i * 1024 + ep + 8); }
      const u32 vo = O_V + j1 * 262144 + kb * 8192 + w * 2048;
      __builtin_amdgcn_raw_buffer_store_b128(e0, rw, lane * 16, vo, AUX_SC1);
      __builtin_amdgcn_raw_buffer_store_b128(e1, rw, lane * 16, vo + 1024, AUX_SC1);
      if (nyq) {
#pragma unroll
        for (int i = 0; i < 4; ++i) wn[i] = pk2(up(wn[i]) * 0.5f);
        v4u dummy = in;
        p2_pair(pc, in, dummy, wn, wn);
        __builtin_amdgcn_raw_buffer_store_b128(in, rw, lane * 16, O_VN + j1 * 1024, AUX_SC1);
      }
    }
    FENCE();
    STAMP(14);
    if (FFT >= 3) {
      v4u gB[4];
      f2v pw[2];
      u32 ax;
      {
        const float ap = (float)(16 * w + c), aq = ap * (1.f / 64), k0 = (float)(4 * q) * aq;
#pragma unroll
        for (int ks = 0; ks < 4; ++ks)
#pragma unroll
          for (int i = 0; i < 4; ++i) { const float t = k0 + (float)(16 * ks + i) * aq; gB[ks][i] = pk(FCOS(t), FSIN(-t)); }
        const float t = (float)(q * (c >> 1)) * 0.125f - ((c & 1) ? 0.25f : 0.f);
        ax = pk2(f2v{FCOS(t), FSIN(-t)} * 0x1p-19f);
#pragma unroll
        for (int e = 0; e < 2; ++e) { const float u = ap * (float)(2 * q + e) * (1.f / 512); pw[e] = f2v{FCOS(u), FSIN(u)}; }
        PIN(gB[0]); PIN(gB[1]); PIN(gB[2]); PIN(gB[3]); PIN(ax); PIN(pw[0]); PIN(pw[1]);
      }
      STAMP(7);
      lwait<2>(L, C_GW, 2);
      STAMP(13);
      const u32 vo = O_V + (2 * w + 8 * q + (c >> 3)) * 8192 + s0 * 32 + (c & 7) * 4;
      const u32 no = w == 3 ? O_VN + (lane & 7) * 1024 + (((s0 >> 6) * 16 + (s0 & 15)) * 4 + ((s0 >> 4) & 3)) * 4 : WS_BYTES;
      u32 vv[8], nv;
      bool bad = true;
      SPIN(s) {
        FENCE();
        if (bad) {
#pragma unroll
          for (int j = 0; j < 8; ++j) vv[j] = __builtin_amdgcn_raw_buffer_load_b32(rw, vo, j * 262144, AUX_SC1);
          nv = __builtin_amdgcn_raw_buffer_load_b32(rw, no, 0, AUX_SC1);
        }
        bad = max(max(max(max(vv[0], vv[1]), max(vv[2], vv[3])), max(max(vv[4], vv[5]), max(vv[6], vv[7]))), nv) == POISON;
        if (!__any(bad)) break;
        __builtin_amdgcn_s_sleep(VSLEEP);
      }
      STAMP(8);
      f2v a[8], y[8];
#pragma unroll
      for (int j = 0; j < 8; ++j) a[j] = up(vv[j]);
      dft8<1>(a, y);
      v4u yq[2];
#pragma unroll
      for (int i = 0; i < 8; ++i) yq[i >> 2][i & 3] = pk2(y[i]);
      const f4v z = {0.f, 0.f, 0.f, 0.f};
      const u32 uw = L_U + (2 * q) * 272 + (16 * w + c) * 4;
#pragma unroll
      for (int i = 0; i < 8; ++i) {
        v4u A = {0u, 0u, 0u, 0u};
        A[i & 3] = ax;
        const f4v d = mfma16(A, yq[i >> 2], z);
        const f2v d0 = {d[0], d[1]}, d1 = {d[2], d[3]};
        *(LAS u32*)(L + uw + i * 2176) = pk2(__builtin_elementwise_fma(f2v{d0.y, d0.x}, f2v{-pw[0].y, pw[0].y}, d0 * pw[0].x));
        *(LAS u32*)(L + uw + i * 2176 + 272) = pk2(__builtin_elementwise_fma(f2v{d1.y, d1.x}, f2v{-pw[1].y, pw[1].y}, d1 * pw[1].x));
      }
      if (w == 3) {
#pragma unroll
        for (int j = 0; j < 8; ++j) a[j] = up((u32)__builtin_amdgcn_readlane(nv, j));
        dft8<1>(a, y);
        *(LAS f4v*)(L + C_NY) = f4v{y[0].x, y[1].x, y[2].x, y[3].x} * 0x1p-19f;
        *(LAS f4v*)(L + C_NY + 16) = f4v{y[4].x, y[5].x, y[6].x, y[7].x} * 0x1p-19f;
      }
      lsync(L, C_H, w, 7);
      const u32 uo = L_U + (c >> 3) * 2176 + (c & 7) * 272 + q * 16;
      const u32 go = O_GA + (s0 + 256 * (q >> 1)) * 1024 + ((16 * w + c) * 8 + 4 * (q & 1)) * 2;
#pragma unroll
      for (int pi = 0; pi < 4; ++pi) {
        const float nyv = *(LAS float*)(L + C_NY + (2 * pi + (q >> 1)) * 4), g = *(LAS float*)(L + C_G + (2 * pi + (q >> 1)) * 4);
        f4v d = {nyv, -nyv, nyv, -nyv};
#pragma unroll
        for (int ks = 0; ks < 4; ++ks) d = mfma16(*(LAS v4u*)(L + uo + pi * 4352 + ks * 64), gB[ks], d);
        const v2u o = {san(pk2(up(pk(d[0], d[1])) * g)), san(pk2(up(pk(d[2], d[3])) * g))};
        __builtin_amdgcn_raw_buffer_store_b64(o, rw, go, pi * 524288, AUX_SC1);
      }
    }
    FENCE();
    STAMP(9);
    {
      const int s = 64 * st + (tid >> 3);
      const u32 go = O_GA + s * 1024 + dt * 128 + (tid & 7) * 16;
      lwait<2>(L, C_GW, 5);
      fin_om = __builtin_amdgcn_raw_buffer_load_b32(rw, O_XN + (s & 255) * 128 + 32 + (s >> 8) * 4, 0, AUX_SC1);
      const u32 o2 = __builtin_amdgcn_raw_buffer_load_b32(rw, O_XN + (s & 255) * 128 + 32 * 128 + 32 + (s >> 8) * 4, 0, AUX_SC1);
      v4u g2;
      SPIN(sp) {
        FENCE();
        fin_ga = __builtin_amdgcn_raw_buffer_load_b128(rw, go, 0, AUX_SC1);
        g2 = __builtin_amdgcn_raw_buffer_load_b128(rw, go + 32768, 0, AUX_SC1);
        if (FFT < 3 || !__any(max(max4(fin_ga), max4(g2)) == POISON)) break;
        __builtin_amdgcn_s_sleep(8);
      }
      *(LAS v4u*)(L + L_GA + tid * 16) = g2;
      *(LAS u32*)(L + L_GA + 4096 + (tid >> 3) * 4) = o2;
    }
    STAMP(10);
  } else {
    INDICES();
    wgbar();
    if (w == 0) dma4(lbase + L_B + 1024, lane * 4, mksrd(p.b2, 4096), st * 128);
    if (!spare) {
      const u32 lb = lbase + L_G + w * 32768, lo = L_G + w * 32768;
      const int hh = lane >> 5, blk = (lane >> 4) & 1, tq = (lane & 15) >> 2, tp = lane & 3;
      const int t1i = 28 * xl + idx, ft = t1i >> 3, d1 = t1i & 7;
      const bool half = (xl & 1) ? idx < 4 : idx >= 24;
      const int rot1 = half ? 2 * (d1 & 3) : d1;
      const v4i s1 = mksrd(p.w1, 2048u * 3584u), s2 = mksrd(p.w2, 1792u * 4096u);
      dma4(lbase + L_B + w * 256, lane * 4, mksrd(p.b1, 3584), (64 * ft + 32 * (w >> 1)) * 2);
      u32 v1off[4];
#pragma unroll
      for (int j = 0; j < 4; ++j) v1off[j] = ((lane >> 3) ^ (j >> 1)) * 3584 + ((lane & 7) ^ ((j & 1) << 2)) * 16;
      auto gblk = [&](int bl) { return (u32)(64 * w + 8 * ((bl + rot1) & 7)); };
      auto w1op = [&](int bl, int slot, int j) {
        if (bl < NTB) dma16<true>(lb + slot * 8192 + j * 1024, v1off[j & 3], s1, gblk(bl) * 3584u + ft * 128 + j * (256u * 3584u));
        else dma16<false>(lb + slot * 8192 + j * 1024, v1off[j & 3], s1, gblk(bl) * 3584u + ft * 128 + j * (256u * 3584u));
      };
      const u32 t1 = lo + tq * 1024 + ((hh ^ (tq >> 1)) * 128) + ((((w >> 1) * 4 + 2 * blk + (tp >> 1)) ^ ((tq & 1) << 2)) * 16) + 8 * (tp & 1);
      const u32 t1x = t1 ^ 64u;
      v4u wr_[24];
      v2u f0;
      sfor<0, 56>([&](auto N) {
        constexpr int n = decltype(N)::v, T = THR < 25 ? THR : 25;
        if constexpr (n == OWNA && OWNA < 8) lwait<1>(L, C_H, 2);
        if constexpr (n == 8) *(volatile LAS u32*)(L + C_GW + w * 4) = 1u;
        if constexpr (n == 8 && OWNWAIT < 8) waitv<OWNWAIT>();
        if constexpr (n >= THR) waitv<THR - 1>();
        if constexpr (n >= 7 + T && (n - 7 - T) % 8 == 0 && (n - 7 - T) / 8 < 3) {
          constexpr int bl = (n - 7 - T) / 8;
          if constexpr (THR > 25) waitv<24>();
#pragma unroll
          for (int k = 0; k < 4; ++k) {
            wr_[8 * bl + 2 * k] = trd2(L, t1 + bl * 8192 + k * 256, t1 + bl * 8192 + k * 256 + 4096);
            wr_[8 * bl + 2 * k + 1] = trd2(L, t1x + bl * 8192 + k * 256, t1x + bl * 8192 + k * 256 + 4096);
          }
          WAITL();
        }
        w1op(n >> 3, (n >> 3) & 3, n & 7);
        if constexpr (n == 15) { STAMP(13); }
        if constexpr (n == 31) { STAMP(1); }
        if constexpr (n == POLLAT) ld8s(f0, O_F1H + (64 * w + lane) * F1HS, sw);
      });
      STAMP(2);
      const v2u tg = *(LAS v2u*)(L + C_TAG);
      asm volatile("s_waitcnt vmcnt(%1)" : "+v"(f0) : "n"(55 - POLLAT) : "memory");
      if (!__all(f0.x == tg.x && f0.y == tg.y)) pollflags<2>((f0.x == tg.x && f0.y == tg.y) ? WS_BYTES : O_F1H + (64 * w + lane) * F1HS, sw, tg);
      STAMP(3);
      const u32 hv[2] = {(u32)lane * 16 + O_H + (2 * d1 + (w & 1)) * 131072, (u32)lane * 16 + O_H + (2 * d1 + ((w & 1) ^ 1)) * 131072};
      v4u act[32][2];
      auto hload = [&](int k, int e) { ld16o((k & 3) * 1024, act[k][e], hv[e], sw, gblk(k >> 2) * 512u); };
#pragma unroll
      for (int k = 0; k < issued(-1, 32); ++k) { hload(k, 0); hload(k, 1); }
      *(volatile LAS u32*)(L + C_OK + w * 4) = 1u;
      const u32 v2off = (lane >> 3) * 4096 + (((lane & 7) ^ (((lane >> 4) & 1) << 2)) * 16);
      auto w2dma = [&](int kl) {
        u32 kg = kl + ((7 * dt) >> 1); kg = kg >= 28 ? kg - 28 : kg;
        const u32 so = (u32)(448 * w) * 4096u + st * 128 + kg * 65536u;
        if (kl < NTW2) { dma16<true>(lb + kl * 2048, v2off, s2, so); dma16<true>(lb + kl * 2048 + 1024, v2off, s2, so + 8 * 4096); }
        else { dma16<false>(lb + kl * 2048, v2off, s2, so); dma16<false>(lb + kl * 2048 + 1024, v2off, s2, so + 8 * 4096); }
      };
      f16v acc[4];
      v4u wf[2][2];
      sfor<0, 32>([&](auto K) {
        constexpr int k = decltype(K)::v, kn = k + 1;
        waitv2<ops_after<1>(k)>(act[k][0], act[k][1]);
        if constexpr (k == 0) { STAMP(4); }
        const v4u a0 = k < 12 ? wr_[(2 * k) % 24] : wf[k & 1][0], a1 = k < 12 ? wr_[(2 * k + 1) % 24] : wf[k & 1][1];
        const f16v z = {};
        constexpr int i0 = issued(k - 1, 32), i1 = issued(k, 32);
        constexpr u32 o = ((kn >> 2) & 3) * 8192 + (kn & 3) * 256;
        acc[0] = mfma32(a0, act[k][0], k ? acc[0] : z);
        SCHED();
        if constexpr (kn >= 12 && kn < 32) wf[kn & 1][0] = trd2(L, t1 + o, t1 + o + 4096);
        if constexpr (i0 < i1) hload(i0, 0);
        SCHED();
        acc[1] = mfma32(a0, act[k][1], k ? acc[1] : z);
        SCHED();
        if constexpr (kn >= 12 && kn < 32) wf[kn & 1][1] = trd2(L, t1x + o, t1x + o + 4096);
        if constexpr (i0 < i1) hload(i0, 1);
        SCHED();
        acc[2] = mfma32(a1, act[k][0], k ? acc[2] : z);
        SCHED();
        if constexpr (i0 + 1 < i1) hload(i0 + 1, 0);
        SCHED();
        acc[3] = mfma32(a1, act[k][1], k ? acc[3] : z);
        SCHED();
        if constexpr (i0 + 1 < i1) hload(i0 + 1, 1);
        if constexpr (k == 15) {
          STAMP(14);
          WAITL();
#pragma unroll
          for (int j = 0; j < 8; ++j) w1op(7, 3, j);
        }
        if constexpr (k >= W2AT && k < W2AT + W2N) w2dma(k - W2AT);
        SCHED();
      });
      STAMP(5);
#pragma unroll
      for (int k = W2N; k < W2PRE; ++k) w2dma(k);
#pragma unroll
      for (int l = 1; l < 4; ++l)
#pragma unroll
        for (int g = 0; g < 4; ++g) {
          const f4v v = {acc[l][4 * g], acc[l][4 * g + 1], acc[l][4 * g + 2], acc[l][4 * g + 3]};
          *(LAS f4v*)(L + lo + 20480 + (l - 1) * 4096 + g * 1024 + lane * 16) = v;
        }
      lsig(L, C_GW, w, 2);
      f4v sum[4];
#pragma unroll
      for (int g = 0; g < 4; ++g) {
        const v2u bb = *(LAS v2u*)(L + L_B + w * 256 + (8 * g + 4 * hh) * 2);
        sum[g] = f4v{acc[0][4 * g], acc[0][4 * g + 1], acc[0][4 * g + 2], acc[0][4 * g + 3]} + f4v{blo(bb.x), bhi(bb.x), blo(bb.y), bhi(bb.y)};
      }
      lwait(L, C_GW, 2);
#pragma unroll
      for (int l = 1; l < 4; ++l)
#pragma unroll
        for (int g = 0; g < 4; ++g) sum[g] += *(LAS f4v*)(L + L_G + (w ^ l) * 32768 + 20480 + (l - 1) * 4096 + g * 1024 + lane * 16);
      lsig(L, C_GW, w, 3);
      v4u uf[2];
#pragma unroll
      for (int g = 0; g < 4; ++g) {
        const f4v v = sum[g];
        const f4v t = v * (v * v * -0.1029432397f - 2.3022081985f);
        const f4v e = f4v{__builtin_amdgcn_exp2f(t[0]), __builtin_amdgcn_exp2f(t[1]), __builtin_amdgcn_exp2f(t[2]), __builtin_amdgcn_exp2f(t[3])} + 1.f;
        const f4v o = v * f4v{__builtin_amdgcn_rcpf(e[0]), __builtin_amdgcn_rcpf(e[1]), __builtin_amdgcn_rcpf(e[2]), __builtin_amdgcn_rcpf(e[3])};
        uf[g >> 1][2 * (g & 1)] = pk(o[0], o[1]); uf[g >> 1][2 * (g & 1) + 1] = pk(o[2], o[3]);
      }
      const u32 uo = O_U + (u32)((2 * d1 + (w & 1)) * 112 + 4 * ft + 2 * (w >> 1)) * 1024u;
      st16s(uf[0], lane * 16, sw, uo);
      st16s(uf[1], lane * 16, sw, uo + 1024);
      STAMP(6);
      lwait(L, C_GW, 3);
      constexpr int K0 = W2PRE > W2N ? W2PRE : W2N;
#pragma unroll
      for (int k = K0; k < K0 + 4; ++k) w2dma(k);
      waitv<8>();
      lsig(L, C_GW, w, 4);
      if (w == 0) { lwait(L, C_GW, 4); st8s(tg, lane == 0 ? O_F2 + (d1 * 28 + ft) * 128 : WS_BYTES, sw); }
      STAMP(7);
#pragma unroll
      for (int k = K0 + 4; k < 16; ++k) w2dma(k);
    } else {
      if (HELP) {
        const int fth = (28 * xl + ((xl & 1) ? 0 : 24)) >> 3;
        dma4nt(lbase + L_D, ((lane & 7) + 256 * (lane >> 3)) * 3584, mksrd(p.w1, 2048u * 3584u), (u32)(64 * w + 8 * (2 * (idx & 3) + 1)) * 3584u + fth * 128);
      }
      lwait<2>(L, C_H, 4);
      const v4i s2 = mksrd(p.w2, 1792u * 4096u);
      const u32 r0 = (u32)(112 * (4 * (idx & 3) + w)) * 4096u + xl * 512;
      __builtin_amdgcn_s_sleep(SPD);
      *(volatile LAS u32*)(L + C_OK + w * 4) = 1u;
#pragma unroll
      for (int i = 0; i < SPN; ++i) {
        if (SPNT) dma4nt(lbase + L_D, (lane >> 2) * 4096 + (lane & 3) * 128, s2, r0 + i * 65536);
        else dma4(lbase + L_D, (lane >> 2) * 4096 + (lane & 3) * 128, s2, r0 + i * 65536);
        waitv<SPW>();
      }
      if (FFT >= 2) lwait<2>(L, C_H, 6);
      __builtin_amdgcn_s_sleep(SPD2);
      lsig(L, C_GW, w, 2);
      STAMP(2);
    }
    {
    INDICES();
    const u32 lb = lbase + L_G + w * 32768, w2b = (u32)(448 * w) * 4096u + st * 128, rot2 = (7 * dt) >> 1;
    const u32 lo = L_G + w * 32768;
    const v4i s2 = mksrd(p.w2, 1792u * 4096u);
    const int hh = lane >> 5, blk = (lane >> 4) & 1, tq = (lane & 15) >> 2, tp = lane & 3;
    const u32 v2off = (lane >> 3) * 4096 + (((lane & 7) ^ (((lane >> 4) & 1) << 2)) * 16);
    auto w2dma = [&](int kl) {
      u32 kg = kl + rot2; kg = kg >= 28 ? kg - 28 : kg;
      dma16<false>(lb + (kl & 15) * 2048, v2off, s2, w2b + kg * 65536u);
      dma16<false>(lb + (kl & 15) * 2048 + 1024, v2off, s2, w2b + kg * 65536u + 8 * 4096);
    };
    const u32 t2 = lo + (4 * hh + tq) * 128 + (((2 * blk + (tp >> 1)) ^ (((tq >> 1) & 1) << 2)) * 16) + 8 * (tp & 1), t2x = t2 ^ 64u;
    if (spare) {
#pragma unroll
      for (int k = 0; k < 16; ++k) w2dma(k);
    }
    v4u wr_[24];
    sfor<0, 3>([&](auto I) {
      constexpr int i = decltype(I)::v;
      waitv<24>();
#pragma unroll
      for (int k = 4 * i; k < 4 * i + 4; ++k) { wr_[2 * k] = trd2(L, t2 + k * 2048, t2 + k * 2048 + 1024); wr_[2 * k + 1] = trd2(L, t2x + k * 2048, t2x + k * 2048 + 1024); }
      WAITL();
#pragma unroll
      for (int k = 4 * i; k < 4 * i + 4; ++k) w2dma(16 + k);
    });
    STAMP(8);
    pollflags<2>(lane < 7 ? O_F2 + (dt * 28 + 7 * w + lane) * 128 : WS_BYTES, sw, *(LAS v2u*)(L + C_TAG));
    STAMP(9);
    const u32 uv[2] = {(u32)lane * 16 + O_U + (2 * dt * 112 + 28 * w) * 1024, (u32)lane * 16 + O_U + ((2 * dt + 1) * 112 + 28 * w) * 1024};
    v4u act[28][2];
    auto uload = [&](int k, int e) {
      u32 kg = k + rot2; kg = kg >= 28 ? kg - 28 : kg;
      ld16o(0, act[k][e], uv[e], sw, kg * 1024u);
    };
#pragma unroll
    for (int k = 0; k < issued(-1, 28); ++k) { uload(k, 0); uload(k, 1); }
    f16v acc[4];
    v4u wf[2][2];
    sfor<0, 28>([&](auto K) {
      constexpr int k = decltype(K)::v, kn = k + 1;
      waitv2<ops_after<2>(k)>(act[k][0], act[k][1]);
      if constexpr (k == 0) { STAMP(10); }
      const v4u b0 = k < 12 ? wr_[(2 * k) % 24] : wf[k & 1][0], b1 = k < 12 ? wr_[(2 * k + 1) % 24] : wf[k & 1][1];
      const f16v z = {};
      constexpr int i0 = issued(k - 1, 28), i1 = issued(k, 28);
      constexpr u32 o = (kn & 15) * 2048;
      acc[0] = mfma32(act[k][0], b0, k ? acc[0] : z);
      SCHED();
      if constexpr (k == GAAT) *(volatile LAS u32*)(L + C_GW + w * 4) = 5u;
      if constexpr (kn >= 12 && kn < 28) wf[kn & 1][0] = trd2(L, t2 + o, t2 + o + 1024);
      if constexpr (i0 < i1) uload(i0, 0);
      SCHED();
      acc[2] = mfma32(act[k][1], b0, k ? acc[2] : z);
      SCHED();
      if constexpr (kn >= 12 && kn < 28) wf[kn & 1][1] = trd2(L, t2x + o, t2x + o + 1024);
      if constexpr (i0 < i1) uload(i0, 1);
      SCHED();
      acc[1] = mfma32(act[k][0], b1, k ? acc[1] : z);
      SCHED();
      if constexpr (i0 + 1 < i1) uload(i0 + 1, 0);
      SCHED();
      acc[3] = mfma32(act[k][1], b1, k ? acc[3] : z);
      SCHED();
      if constexpr (i0 + 1 < i1) uload(i0 + 1, 1);
      SCHED();
    });
    STAMP(11);
#pragma unroll
    for (int md = 0; md < 2; ++md)
#pragma unroll
      for (int nt = 0; nt < 2; ++nt)
#pragma unroll
        for (int g = 0; g < 4; ++g) {
          const f16v& a = acc[2 * md + nt];
          const f4v v = {a[4 * g], a[4 * g + 1], a[4 * g + 2], a[4 * g + 3]};
          *(LAS f4v*)(L + lo + (32 * nt + (lane & 31)) * 272 + (32 * md + 8 * g + 4 * hh) * 4) = v;
        }
    STAMP(12);
    }
  }
  wgbar();
  {
    INDICES();
    const int sl = tid >> 3;
    const u32 ro = L_G + sl * 272 + (tid & 7) * 32;
    f4v s0v = *(LAS f4v*)(L + ro), s1v = *(LAS f4v*)(L + ro + 16);
#pragma unroll
    for (int l = 1; l < 4; ++l) { s0v += *(LAS f4v*)(L + ro + l * 32768); s1v += *(LAS f4v*)(L + ro + l * 32768 + 16); }
    if (wv >= 4) { fin_ga = *(LAS v4u*)(L + L_GA + (tid - 256) * 16); fin_om = *(LAS u32*)(L + L_GA + 4096 + (sl - 32) * 4); }
    const float b2 = blo(*(LAS u16*)(L + L_B + 1024 + sl * 2)), om = __uint_as_float(fin_om);
    v4u o;
#pragma unroll
    for (int i = 0; i < 4; ++i) {
      const f2v e = i < 2 ? f2v{s0v[2 * i], s0v[2 * i + 1]} : f2v{s1v[2 * i - 4], s1v[2 * i - 3]};
      o[i] = pk2(up(fin_ga[i]) + up(pk2(up(pk2(up(pk2(e)) + b2)) * om)));
    }
    *(v4u*)(p.out + (size_t)(64 * st + sl) * 512 + dt * 64 + (tid & 7) * 8) = o;
  }
}

extern "C" void kernel_launch(void* const* d_in, const int* in_sizes, int n_in, void* d_out, int out_size, void* d_ws, size_t ws_size,
                              hipStream_t stream) {
  Params p;
  p.x = (const u16*)d_in[0]; p.wr = (const u16*)d_in[1]; p.wi = (const u16*)d_in[2];
  p.w1 = (const u16*)d_in[4]; p.b1 = (const u16*)d_in[5]; p.w2 = (const u16*)d_in[6]; p.b2 = (const u16*)d_in[7]; p.gw = (const u16*)d_in[8];
  p.out = (u16*)d_out;
  p.ws = (char*)d_ws + ((ws_size - WS_BYTES) & ~(size_t)0xFFFFF);
  (void)hipFuncSetAttribute((const void*)fused_kernel, hipFuncAttributeMaxDynamicSharedMemorySize, LDS_BYTES);
  fused_kernel<<<256, 512, LDS_BYTES, stream>>>(p);
}
```

```cpp
#include <hip/hip_runtime.h>
#include <stdint.h>

typedef unsigned u32;
typedef unsigned long long u64;
typedef unsigned short u16;
typedef u32 v4u __attribute__((ext_vector_type(4)));
typedef u32 v2u __attribute__((ext_vector_type(2)));
typedef int v4i __attribute__((ext_vector_type(4)));
typedef short s4v __attribute__((ext_vector_type(4)));
typedef short s8v __attribute__((ext_vector_type(8)));
typedef float f2v __attribute__((ext_vector_type(2)));
typedef float f4v __attribute__((ext_vector_type(4)));
typedef float f16v __attribute__((ext_vector_type(16)));
typedef __bf16 b2v __attribute__((ext_vector_type(2)));
#define LAS __attribute__((address_space(3)))
#define DI __device__ __forceinline__
typedef __amdgpu_buffer_rsrc_t rsrc_t;

constexpr u32 O_EPOCH = 0, O_F1X = 106496  , O_F2 = 32768, O_F1H = 147456, O_F3 = 180224, O_F4 = 212992, O_XN = 65536  , O_VN = 98304, O_DIAG = 262144;
constexpr u32 O_H = 1u << 20, O_X1 = 3u << 20, O_V = 5u << 20, O_GA = 7u << 20, O_U = 9u << 20, WS_BYTES = 11u << 20;
constexpr u32 POISON = 0xFFFFFFFFu;
#define USENT 0
constexpr int AUX_SC1 = 16, AUX_NT = USENT ? 2 : 0;
constexpr u32 SPIN_CAP = 100000;
constexpr u32 L_G = 0;
constexpr u32 L_S = 131072;
constexpr u32 L_X = L_S, L_HR = L_S + 8704, L_T = L_S + 17408;
constexpr u32 L_E = L_S, L_W = L_S + 8704, L_O = L_S + 17408;
constexpr u32 L_U = L_S;
constexpr u32 L_GA = L_S + 17408;
constexpr u32 L_C = L_S + 26624;
constexpr u32 L_B = L_C + 256, L_D = L_C + 1536;
constexpr u32 LDS_BYTES = 163840;
constexpr u32 C_H = L_C, C_GW = L_C + 16, C_TAG = L_C + 32, C_OK = L_C + 48, C_G = L_C + 64, C_NY = L_C + 96;

struct Params {
  const u16 *x, *wr, *wi, *w1, *b1, *w2, *b2, *gw;
  u16* out;
  char* ws;
};

#define THR 32
#define DMAX 8
#define NTB 1
#define NTW2 4
#define W2AT 23
#define W2NMAX 4
#define GAAT 12
#define GOAT 47
#define SPD 8
#define SPN 0
#define WGAT 1
#define FFT 3
#define HELP 1
#define OWNWAIT 0
#define SPPH 2
#define SPNT 0
#define SPW 1
#define SPGO 0
#define OWNTHR 8
#define F1XS 128
#define POLLAT 32
#define OWNA 4
#define XDLY 0
#define HACK 0
#define SPD2 64
#define CONSTA 1
#define F1HS 8
#define W2PRE 0
#define F3S 8
#define F4S 128
#define P3AT 2
#define P2AT 0
#define MAP1 0
#define MAP2 0
#define GDLY 0
#define CODEPF 36000
#define POLLUSE 48
#define W2PRE2 0
#define VSLEEP 8

DI u32 pk(float a, float b) { f2v v = {a, b}; return __builtin_bit_cast(u32, __builtin_convertvector(v, b2v)); }
DI u32 pk2(f2v v) { return __builtin_bit_cast(u32, __builtin_convertvector(v, b2v)); }
DI float blo(u32 v) { return __uint_as_float(v << 16); }
DI float bhi(u32 v) { return __uint_as_float(v & 0xFFFF0000u); }
DI f2v up(u32 v) { const f2v r = {blo(v), bhi(v)}; return r; }
DI float rbf(float a) { return blo(pk(a, a)); }
DI u32 san(u32 v) { return v < 0xFFFFFFFEu ? v : 0xFFFFFFFEu; }
DI u32 max4(v4u v) { return max(max(v.x, v.y), max(v.z, v.w)); }
DI f4v mfma16(v4u a, v4u b, f4v c) { return __builtin_amdgcn_mfma_f32_16x16x32_bf16(__builtin_bit_cast(s8v, a), __builtin_bit_cast(s8v, b), c, 0, 0, 0); }
DI f16v mfma32_real(v4u a, v4u b, f16v c) { return __builtin_amdgcn_mfma_f32_32x32x16_bf16(__builtin_bit_cast(s8v, a), __builtin_bit_cast(s8v, b), c, 0, 0, 0); }
#define mfma32 mfma32_real
DI v2u trd(LAS char* L, u32 off) { return __builtin_bit_cast(v2u, __builtin_amdgcn_ds_read_tr16_b64_v4i16((LAS s4v*)(L + off))); }
DI v4u trd2(LAS char* L, u32 o0, u32 o1) { v2u a = trd(L, o0), b = trd(L, o1); v4u r = {a.x, a.y, b.x, b.y}; return r; }
template <int C> DI float dppadd(float v) { return v + __int_as_float(__builtin_amdgcn_update_dpp(0, __float_as_int(v), C, 0xF, 0xF, true)); }
DI float dsum16(float v) { v = dppadd<0xB1>(v); v = dppadd<0x4E>(v); v = dppadd<0x141>(v); return dppadd<0x140>(v); }
DI float sw16(float a, float b) { auto r = __builtin_amdgcn_permlane16_swap(__float_as_uint(a), __float_as_uint(b), false, false); return __uint_as_float(r[0]) + __uint_as_float(r[1]); }
DI float sw32(float a, float b) { auto r = __builtin_amdgcn_permlane32_swap(__float_as_uint(a), __float_as_uint(b), false, false); return __uint_as_float(r[0]) + __uint_as_float(r[1]); }
DI float rdl(float v, int l) { return __int_as_float(__builtin_amdgcn_readlane(__float_as_int(v), l)); }
DI u64 mix64(u64 z) { z += 0x9E3779B97F4A7C15ull; z = (z ^ (z >> 30)) * 0xBF58476D1CE4E5B9ull; z = (z ^ (z >> 27)) * 0x94D049BB133111EBull; return z ^ (z >> 31); }
#define WAITL() asm volatile("s_waitcnt lgkmcnt(0)" ::: "memory")
template <int N> DI void waitv() { asm volatile("s_waitcnt vmcnt(%0)" ::"n"(N) : "memory"); }
#define SCHED() __builtin_amdgcn_sched_barrier(0)
#define FENCE() asm volatile("" ::: "memory")
#define PIN(x) asm volatile("" : "+v"(x))
#define SPIN(var) _Pragma("unroll 1") for (u32 var = 0; var < SPIN_CAP; ++var)
#define FCOS __builtin_amdgcn_cosf
#define FSIN __builtin_amdgcn_sinf
DI void lsig(LAS char* L, u32 slot, int w, u32 ph) { WAITL(); *(volatile LAS u32*)(L + slot + w * 4) = ph; }
template <int SL = 0> DI void lwait(LAS char* L, u32 slot, u32 ph) {
  _Pragma("unroll 1") for (u32 s = 0; s < 40u * SPIN_CAP; ++s) {
    FENCE();
    const v4u t = *(LAS v4u*)(L + slot);
    if ((u32)__builtin_amdgcn_readfirstlane(min(min(t.x, t.y), min(t.z, t.w))) >= ph) break;
    if (SL) __builtin_amdgcn_s_sleep(SL);
  }
  FENCE();
}
DI void lwait1(LAS char* L, u32 a, u32 ph) {
  _Pragma("unroll 1") for (u32 s = 0; s < 40u * SPIN_CAP && (u32)__builtin_amdgcn_readfirstlane(*(volatile LAS u32*)(L + a)) < ph; ++s) __builtin_amdgcn_s_sleep(1);
  FENCE();
}
DI void lsync(LAS char* L, u32 slot, int w, u32 ph) { lsig(L, slot, w, ph); lwait(L, slot, ph); }
DI void wgbar() { WAITL(); __builtin_amdgcn_s_barrier(); FENCE(); }

#define STAMP(i)

template <int SGN> DI f2v addi(f2v a, f2v b) { const f2v t = {b.y, b.x}, s = {-(float)SGN, (float)SGN}; return __builtin_elementwise_fma(t, s, a); }
template <int SGN> DI void dft8(const f2v (&a)[8], f2v (&y)[8]) {
  const float R = 0.70710678118654752f;
  const f2v b0 = a[0] + a[4], b4 = a[0] - a[4], b1 = a[1] + a[5], d1 = a[1] - a[5], b2 = a[2] + a[6], d2 = a[2] - a[6], b3 = a[3] + a[7], d3 = a[3] - a[7];
  const f2v b5 = addi<SGN>(d1, d1) * R, b7 = addi<-SGN>(d3, d3) * (-R);
  const f2v e = b0 + b2, f = b0 - b2, g = b1 + b3, h = b1 - b3;
  y[0] = e + g; y[4] = e - g; y[2] = addi<SGN>(f, h); y[6] = addi<-SGN>(f, h);
  const f2v e2 = addi<SGN>(b4, d2), f2 = addi<-SGN>(b4, d2), g2 = b5 + b7, h2 = b5 - b7;
  y[1] = e2 + g2; y[5] = e2 - g2; y[3] = addi<SGN>(f2, h2); y[7] = addi<-SGN>(f2, h2);
}
DI v4u cmulpk(f4v r, f4v m, f4v C, f4v S) {
  const f4v a = r * C - m * S, b = m * C + r * S;
  const v4u t = {pk(a[0], b[0]), pk(a[1], b[1]), pk(a[2], b[2]), pk(a[3], b[3])};
  return t;
}
struct P2C { v4u f1r, f1i, fr, fi, gr, gi, g1r, g1i; f4v tc, ts, uc, us; };
DI void p2_pair(const P2C& c, v4u& a, v4u& b, v4u wa, v4u wb) {
  const f4v z = {0.f, 0.f, 0.f, 0.f};
  f4v r0 = mfma16(a, c.f1r, z), m0 = mfma16(a, c.f1i, z), r1 = mfma16(b, c.f1r, z), m1 = mfma16(b, c.f1i, z);
  const f4v ca = {blo(wa[0]), blo(wa[1]), blo(wa[2]), blo(wa[3])}, sa = {bhi(wa[0]), bhi(wa[1]), bhi(wa[2]), bhi(wa[3])};
  SCHED();
  a = cmulpk(r0, m0, c.tc, c.ts); r0 = mfma16(c.fr, a, z); m0 = mfma16(c.fi, a, z);
  SCHED();
  b = cmulpk(r1, m1, c.tc, c.ts); r1 = mfma16(c.fr, b, z); m1 = mfma16(c.fi, b, z);
  SCHED();
  const f4v cb = {blo(wb[0]), blo(wb[1]), blo(wb[2]), blo(wb[3])}, sb = {bhi(wb[0]), bhi(wb[1]), bhi(wb[2]), bhi(wb[3])};
  a = cmulpk(r0, m0, ca, sa); r0 = mfma16(a, c.gr, z); m0 = mfma16(a, c.gi, z);
  SCHED();
  b = cmulpk(r1, m1, cb, sb); r1 = mfma16(b, c.gr, z); m1 = mfma16(b, c.gi, z);
  SCHED();
  a = cmulpk(r0, m0, c.uc, c.us); r0 = mfma16(c.g1r, a, z); m0 = mfma16(c.g1i, a, z);
  SCHED();
  b = cmulpk(r1, m1, c.uc, c.us); r1 = mfma16(c.g1r, b, z); m1 = mfma16(c.g1i, b, z);
  SCHED();
#pragma unroll
  for (int i = 0; i < 4; ++i) a[i] = pk(r0[i], m0[i]);
#pragma unroll
  for (int i = 0; i < 4; ++i) b[i] = pk(r1[i], m1[i]);
}

template <bool NT> DI void dma16(u32 ldsaddr, u32 voff, v4i srd, u32 soff) {
  if constexpr (NT && USENT) asm volatile("s_mov_b32 m0, %0\n\ts_nop 0\n\tbuffer_load_dwordx4 %1, %2, %3 offen nt lds" ::"s"(ldsaddr), "v"(voff), "s"(srd), "s"(soff) : "memory");
  else asm volatile("s_mov_b32 m0, %0\n\ts_nop 0\n\tbuffer_load_dwordx4 %1, %2, %3 offen lds" ::"s"(ldsaddr), "v"(voff), "s"(srd), "s"(soff) : "memory");
}
DI void dma4(u32 ldsaddr, u32 voff, v4i srd, u32 soff) {
  asm volatile("s_mov_b32 m0, %0\n\ts_nop 0\n\tbuffer_load_dword %1, %2, %3 offen lds" ::"s"(ldsaddr), "v"(voff), "s"(srd), "s"(soff) : "memory");
}
DI void dma4nt(u32 ldsaddr, u32 voff, v4i srd, u32 soff) {
  if constexpr (USENT) asm volatile("s_mov_b32 m0, %0\n\ts_nop 0\n\tbuffer_load_dword %1, %2, %3 offen nt lds" ::"s"(ldsaddr), "v"(voff), "s"(srd), "s"(soff) : "memory");
  else dma4(ldsaddr, voff, srd, soff);
}
DI void ld16o(int imm, v4u& d, u32 voff, v4i srd, u32 soff) { asm volatile("buffer_load_dwordx4 %0, %1, %2, %3 offen offset:%4 sc1" : "=v"(d) : "v"(voff), "s"(srd), "s"(soff), "n"(imm) : "memory"); }
DI void ld8s(v2u& d, u32 voff, v4i srd) { asm volatile("buffer_load_dwordx2 %0, %1, %2, 0 offen sc1" : "=v"(d) : "v"(voff), "s"(srd) : "memory"); }
DI v2u ld8w(u32 voff, v4i srd) { v2u d; asm volatile("buffer_load_dwordx2 %0, %1, %2, 0 offen sc1\n\ts_waitcnt vmcnt(0)" : "=&v"(d) : "v"(voff), "s"(srd) : "memory"); return d; }
DI void st16s(v4u d, u32 voff, v4i srd, u32 soff) { asm volatile("buffer_store_dwordx4 %0, %1, %2, %3 offen sc1\n\ts_nop 1" ::"v"(d), "v"(voff), "s"(srd), "s"(soff) : "memory"); }
DI void st8s(v2u d, u32 voff, v4i srd) { asm volatile("buffer_store_dwordx2 %0, %1, %2, 0 offen sc1\n\ts_nop 1" ::"v"(d), "v"(voff), "s"(srd) : "memory"); }
template <int N> DI void waitv2(v4u& a, v4u& b) { asm volatile("s_waitcnt vmcnt(%2)" : "+v"(a), "+v"(b) : "n"(N) : "memory"); }
template <int N> DI void waitv1(v2u& a) { asm volatile("s_waitcnt vmcnt(%1)" : "+v"(a) : "n"(N) : "memory"); }
DI v4i mksrd(const void* p, u32 bytes) { const u64 a = (u64)p; v4i r = {(int)(u32)a, (int)((u32)(a >> 32) & 0xFFFFu), (int)bytes, 0x00020000}; return r; }
template <int SL> DI void pollflags(u32 off, v4i sw, v2u tg) {
  SPIN(s) {
    const v2u f = ld8w(off, sw);
    if (f.x == tg.x && f.y == tg.y) off = WS_BYTES;
    if (__all(off == WS_BYTES)) break;
    __builtin_amdgcn_s_sleep(SL);
  }
}

constexpr int R0 = 6;
constexpr int imin(int a, int b) { return a < b ? a : b; }
constexpr int issued(int k, int n) { return imin(n, imin(R0 + 2 * (k + 1), k + 1 + DMAX)); }
constexpr int W2N = imin(32 - W2AT, W2NMAX);
constexpr int g1_extra(int k) { return (k == 15 ? 8 : 0) + (k >= W2AT && k < W2AT + W2N ? 2 : 0); }
template <int G> constexpr int ops_after(int k) {
  const int n = G == 1 ? 32 : 28;
  int c = 0; bool seen = false;
  for (int j = -1; j < k; ++j) {
    for (int s = (j < 0 ? 0 : issued(j - 1, n)); s < issued(j, n); ++s) { if (seen) c += 2; if (s == k) seen = true; }
    if (seen && j >= 0 && G == 1) c += g1_extra(j);
  }
  return c;
}
template <int K> struct IC { static constexpr int v = K; };
template <int B, int E, class Fn> DI void sfor(Fn&& f) { if constexpr (B < E) { f(IC<B>{}); sfor<B + 1, E>(f); } }

#define INDICES() \
  int wv = wv_, b = b_, tid = tid_; \
  asm volatile("" : "+s"(wv), "+s"(b), "+v"(tid)); \
  const int lane = tid & 63, w = wv & 3, xl = b & 7, idx = b >> 3, dt = MAP2 ? 4 * (xl >> 2) + (idx & 3) : (idx & 7), st = MAP2 ? 8 * (xl & 3) + (idx >> 2) : 4 * xl + (idx >> 3), s0 = 32 * xl + idx, q = lane >> 4, c = lane & 15; \
  (void)dt; (void)st; (void)s0; (void)q; (void)c; (void)w; (void)xl;

__global__ void __launch_bounds__(512, 2) fused_kernel(Params p) {
  extern __shared__ __attribute__((aligned(16))) char smem[];
  LAS char* L = (LAS char*)smem;
  const int tid_ = threadIdx.x, wv_ = __builtin_amdgcn_readfirstlane(tid_ >> 6), b_ = blockIdx.x;
  const bool spare = b_ >= 224;
  STAMP(0);
  asm volatile("" ::"s"(p.x), "s"(p.wr), "s"(p.wi), "s"(p.w1), "s"(p.b1), "s"(p.w2), "s"(p.b2), "s"(p.gw), "s"(p.out), "s"(p.ws));
  const rsrc_t rw = __builtin_amdgcn_make_buffer_rsrc((void*)p.ws, 0, (int)WS_BYTES, 0x00020000);
  const v4i sw = mksrd(p.ws, WS_BYTES);
  const u32 lbase = (u32)(size_t)smem;

  {
    INDICES();
    dma16<true>(lbase + L_X + wv * 1088, lane * 16, mksrd(p.x, 2048 * 512 * 2), (s0 + 256 * wv) * 1024);
    if (wv >= 4 && !spare) {
      const int t1i = 28 * xl + idx, ft = MAP1 ? 7 * (xl & 3) + (idx >> 2) : t1i >> 3, d1 = MAP1 ? 4 * (xl >> 2) + (idx & 3) : t1i & 7;
      const bool half = MAP1 || ((xl & 1) ? idx < 4 : idx >= 24);
      const int rot1 = half ? 2 * (d1 & 3) : d1, xo1 = MAP1 ? xl >> 2 : 0;
      const u32 so = (u32)(64 * w + 8 * ((xo1 + rot1) & 7)) * 3584u + ft * 128;
#pragma unroll
      for (int j = 0; j < 4; ++j)
        dma16<true>(lbase + L_G + w * 32768 + j * 1024, ((lane >> 3) ^ (j >> 1)) * 3584 + ((lane & 7) ^ ((j & 1) << 2)) * 16, mksrd(p.w1, 2048u * 3584u), so + j * (256u * 3584u));
    } else {
      u64 pc; asm volatile("s_getpc_b64 %0" : "=s"(pc));
      if (wv == 0) dma4(lbase + L_D + 256, lane * 4, mksrd(p.ws + O_EPOCH, 8), 0);
      else if (wv == 1) dma4(lbase + L_D, (lane & 15) * 4096 + idx * 128, mksrd((const void*)(pc & ~(u64)127), CODEPF), 0);
      else if (wv < 4) dma4nt(lbase + L_D, FFT >= 2 ? ((u32)(xl + 8 * (8 * idx + (lane >> 3))) * 514u + min((lane & 7) * 128, 512)) & ~3u : 0x7F000000u, mksrd(w == 2 ? p.wr : p.wi, 2048 * 257 * 2), 0);
      else dma4nt(lbase + L_D, HELP && !MAP1 ? ((lane & 7) + 256 * (lane >> 3)) * 3584 : 0x7F000000u, mksrd(p.w1, 2048u * 3584u),
                  (u32)(64 * w + 8 * (2 * (idx & 3) + 1)) * 3584u + ((28 * xl + ((xl & 1) ? 0 : 24)) >> 3) * 128);
    }
    if (wv < 4 && FFT >= 1) {
      const v4u P = {POISON, POISON, POISON, POISON};
      st16s(P, lane * 16, sw, O_X1 + (2 * w) * 262144 + s0 * 1024);
      st16s(P, lane * 16, sw, O_X1 + (2 * w + 1) * 262144 + s0 * 1024);
      st16s(P, tid < 2 ? O_XN + s0 * 128 + lane * 16 : WS_BYTES, sw, 0);
    }
    if (tid < 16) ((LAS u32*)(L + L_C))[tid] = 0;
    if (wv >= 4 && spare) waitv<1>(); else if (wv >= 4 || FFT >= 1) waitv<4>(); else waitv<1>();
    STAMP(1);
    const v4u xr = *(LAS v4u*)(L + L_X + wv * 1088 + lane * 16);
    f2v xa[4], da[4];
#pragma unroll
    for (int i = 0; i < 4; ++i) xa[i] = up(xr[i]);
    const float pa = rdl(xa[0].x, 0);
#pragma unroll
    for (int i = 0; i < 4; ++i) da[i] = xa[i] - pa;
    const f2v s1 = (da[0] + da[1]) + (da[2] + da[3]), s2 = da[0] * da[0] + da[1] * da[1] + (da[2] * da[2] + da[3] * da[3]);
    float t = sw16(dsum16(s1.x + s1.y), dsum16(s2.x + s2.y));
    t = sw32(t, t);
    const float m1 = rdl(t, 0) * (1.f / 512.f), m2 = rdl(t, 16) * (1.f / 512.f);
    const float rs = __builtin_amdgcn_rcpf(rbf(__builtin_amdgcn_sqrtf(rbf(rbf(fmaxf(m2 - m1 * m1, 0.f)) + 1e-5f))));
    const float nm = -rbf(m1 + pa) * rs;
    v4u ha;
#pragma unroll
    for (int i = 0; i < 4; ++i) ha[i] = pk2(xa[i] * rs + nm);
    *(LAS v4u*)(L + L_HR + wv * 1088 + lane * 16) = ha;
    wgbar();
    STAMP(11);
  }
  v4u fin_ga; u32 fin_om;
  if (wv_ < 4) {
    INDICES();
    __builtin_amdgcn_s_setprio(3);
    const int j1 = xl, kb = idx;
    v4u f64r[2], f64i[2], a8;
    f4v twc, tws;
    auto constA = [&]() {
      const float k1 = (float)(16 * w + c), kq = k1 * (1.f / 64), a0 = (float)(8 * q) * kq;
#pragma unroll
      for (int ks = 0; ks < 2; ++ks)
#pragma unroll
        for (int j = 0; j < 4; ++j) {
          const float t0 = a0 + (float)(32 * ks + 2 * j) * kq, t1 = t0 + kq;
          f64r[ks][j] = pk(FCOS(t0), FCOS(t1));
          f64i[ks][j] = pk(FSIN(-t0), FSIN(-t1));
        }
      PIN(f64r[0]); PIN(f64r[1]); PIN(f64i[0]); PIN(f64i[1]);
    };
    {
      const u32 ta = L_HR + (c >> 2) * 1088 + (128 * w + 16 * q + 4 * (c & 3)) * 2;
      const v4u h0 = trd2(L, ta, ta + 4 * 1088), h1 = trd2(L, ta + 128, ta + 128 + 4 * 1088);
      const u32 vo = (lane >> 5) * 131072 + (lane & 31) * 16;
      __builtin_amdgcn_raw_buffer_store_b128(h0, rw, vo, O_H + (4 * w) * 131072 + s0 * 512, AUX_SC1);
      __builtin_amdgcn_raw_buffer_store_b128(h1, rw, vo, O_H + (4 * w + 2) * 131072 + s0 * 512, AUX_SC1);
    }
    const rsrc_t rr = __builtin_amdgcn_make_buffer_rsrc((void*)p.wr, 0, 2048 * 257 * 2, 0x00020000);
    const rsrc_t ri = __builtin_amdgcn_make_buffer_rsrc((void*)p.wi, 0, 2048 * 257 * 2, 0x00020000);
    const u32 wof = (u32)(j1 + 8 * (64 * w + lane)) * 514u + kb * 16;
    v4u wq0, wq1, wn = {0u, 0u, 0u, 0u};
    const bool nyq = kb == 31 && w == 3;
    auto wgather = [&]() {
      wq0 = __builtin_amdgcn_raw_buffer_load_b128(rr, wof, 0, 0); wq1 = __builtin_amdgcn_raw_buffer_load_b128(ri, wof, 0, 0);
      if (nyq) {
#pragma unroll
        for (int i = 0; i < 4; ++i) {
          const u32 o2 = (u32)(j1 + 8 * (64 * q + 16 * i + c)) * 514u + 512u;
          wn[i] = (u32)__builtin_amdgcn_raw_buffer_load_b16(rr, o2, 0, 0) | ((u32)__builtin_amdgcn_raw_buffer_load_b16(ri, o2, 0, 0) << 16);
        }
      }
    };
    auto wimage = [&]() {
      const u32 epos = ((w * 16 + c) * 4 + q) * 4;
#pragma unroll
      for (int i = 0; i < 4; ++i) {
        u32 lo = __builtin_amdgcn_perm(wq1[i], wq0[i], 0x05040100u), hi = __builtin_amdgcn_perm(wq1[i], wq0[i], 0x07060302u);
        if (i == 0 && kb == 0) lo = pk2(up(lo) * 0.5f);
        *(LAS u32*)(L + L_W + (2 * i) * 1024 + epos) = lo;
        *(LAS u32*)(L + L_W + (2 * i + 1) * 1024 + epos) = hi;
      }
    };
    FENCE();
    *(volatile LAS u32*)(L + C_H + w * 4) = 2u;
    STAMP(2);
    const v4u gwv = *(const v4u*)(p.gw + lane * 8);
    FENCE();
    FENCE();
    constA();
    {
      const float k1 = (float)(16 * w + c), b0 = (float)(4 * (q & 1)), k2 = (float)(c >> 2) * 0.125f, ph = (c & 1) ? 0.25f : 0.f, kt = k1 * (-1.f / 512);
      const bool on = ((c >> 1) & 1) == (q >> 1);
#pragma unroll
      for (int r = 0; r < 4; ++r) {
        const float bb = b0 + (float)r, t = bb * k2 + ph;
        const u32 e = pk(FCOS(t), FSIN(t));
        a8[r] = on ? e : 0u;
        twc[r] = FCOS(bb * kt); tws[r] = FSIN(bb * kt);
      }
    }
    v2u tg;
    {
      PIN(a8); PIN(twc); PIN(tws);
      waitv<1>();
      if (w == 0) {
        const v2u epv = *(LAS v2u*)(L + L_D + 256);
        const u64 tag = mix64(((u64)(u32)__builtin_amdgcn_readfirstlane(epv.y) << 32) | (u32)__builtin_amdgcn_readfirstlane(epv.x));
        *(LAS v2u*)(L + C_TAG) = v2u{(u32)tag, (u32)(tag >> 32)};
      }
      lsync(L, C_H, w, 3);
      tg = *(LAS v2u*)(L + C_TAG);
      if (w == 1 && lane == 0) __builtin_amdgcn_raw_buffer_store_b64(tg, rw, O_F1H + s0 * F1HS, 0, AUX_SC1);
      STAMP(3);
    }
    v4u o0, o1;
    f2v ny[8];
    if (FFT >= 1) {
      f2v y[8], zz[8];
      const f4v z = {0.f, 0.f, 0.f, 0.f};
      const u32 xo = L_X + ((c >> 1) & 1) * 1088 + (8 * q + (c >> 2)) * 16 + 8 * (c & 1);
      v4u a8n = a8; a8n[1] ^= 0x80008000u; a8n[3] ^= 0x80008000u;
#pragma unroll
      for (int pi = 0; pi < 4; ++pi) {
        const v4u A0 = trd2(L, xo + pi * 2176, xo + pi * 2176 + 64), A1 = trd2(L, xo + pi * 2176 + 512, xo + pi * 2176 + 576);
        const v4u t = cmulpk(mfma16(A1, f64r[1], mfma16(A0, f64r[0], z)), mfma16(A1, f64i[1], mfma16(A0, f64i[0], z)), twc, tws);
        const f4v d = mfma16(a8, t, z), e = mfma16(a8n, t, z);
        y[2 * pi] = f2v{d[0], d[1]}; y[2 * pi + 1] = f2v{d[2], d[3]};
        ny[2 * pi] = f2v{e[0], e[1]}; ny[2 * pi + 1] = f2v{e[2], e[3]};
      }
      dft8<-1>(y, zz);
#pragma unroll
      for (int j = 0; j < 8; ++j) *(LAS u32*)(L + L_T + w * 2048 + j * 256 + lane * 4) = san(pk2(zz[j]));
      o0 = *(LAS v4u*)(L + L_T + w * 2048 + lane * 16); o1 = *(LAS v4u*)(L + L_T + w * 2048 + 1024 + lane * 16);
    }
    if (FFT >= 1) {
      f2v zz[8];
      const u32 vo = q * 262144 + (((lane >> 2) & 3) * 64 + (lane & 3) * 4) * 4;
      __builtin_amdgcn_raw_buffer_store_b128(o0, rw, vo, O_X1 + s0 * 1024 + w * 64, AUX_SC1);
      __builtin_amdgcn_raw_buffer_store_b128(o1, rw, vo, O_X1 + 4 * 262144 + s0 * 1024 + w * 64, AUX_SC1);
      if (w == 0) {
        dft8<-1>(ny, zz);
        if (lane == 0) {
          const v4u n0 = {san(pk2(zz[0])), san(pk2(zz[1])), san(pk2(zz[2])), san(pk2(zz[3]))}, n1 = {san(pk2(zz[4])), san(pk2(zz[5])), san(pk2(zz[6])), san(pk2(zz[7]))};
          __builtin_amdgcn_raw_buffer_store_b128(n0, rw, O_XN + s0 * 128, 0, AUX_SC1);
          __builtin_amdgcn_raw_buffer_store_b128(n1, rw, O_XN + s0 * 128 + 16, 0, AUX_SC1);
        }
      }
    }
    if (FFT >= 2 && WGAT == 0) wgather();
    FENCE();
    STAMP(12);
    {
      float ga = 0.f, gb = 0.f;
      const v4u x0 = *(LAS v4u*)(L + L_X + (2 * w) * 1088 + lane * 16), x1 = *(LAS v4u*)(L + L_X + (2 * w + 1) * 1088 + lane * 16);
      const b2v one = {(__bf16)1.f, (__bf16)1.f};
#pragma unroll
      for (int i = 0; i < 4; ++i) {
        const f2v g = up(gwv[i]);
        ga = __builtin_amdgcn_fdot2_f32_bf16(__builtin_convertvector(up(x0[i]) * g, b2v), one, ga, false);
        gb = __builtin_amdgcn_fdot2_f32_bf16(__builtin_convertvector(up(x1[i]) * g, b2v), one, gb, false);
      }
      float t = sw16(dsum16(ga), dsum16(gb));
      t = rbf(sw32(t, t));
      const float g = rbf(__builtin_amdgcn_rcpf(1.f + __builtin_amdgcn_exp2f(-1.44269504088896f * t)));
      if ((lane & 47) == 0) {
        *(LAS float*)(L + C_G + (2 * w + q) * 4) = g;
        __builtin_amdgcn_raw_buffer_store_b32(__float_as_uint(rbf(1.f - g)), rw, O_XN + s0 * 128 + 32 + (2 * w + q) * 4, 0, AUX_SC1);
      }
    }
    if (FFT >= 2 && WGAT == 1) wgather();
    P2C pc;
    {
      const float cf = (float)c, jf = (float)j1, pj = (8.f * cf + jf) * (1.f / 128), e0 = (float)(4 * q);
#pragma unroll
      for (int i = 0; i < 4; ++i) {
        const float e = e0 + (float)i;
        { const float t = e * pj, cs = FCOS(t), sn = FSIN(t); pc.f1r[i] = pk(cs, sn); pc.f1i[i] = pk(-sn, cs); pc.tc[i] = FCOS(t * -0.0625f); pc.ts[i] = FSIN(t * -0.0625f); }
        { const float t = e * cf * 0.0625f, cs = FCOS(t), sn = FSIN(t); pc.fr[i] = pk(cs, sn); pc.fi[i] = pk(-sn, cs); pc.gr[i] = pk(cs, -sn); pc.gi[i] = pk(sn, cs); }
        { const float t = cf * (8.f * e + jf) * (1.f / 128), cs = FCOS(t), sn = FSIN(t); pc.g1r[i] = pk(cs, -sn); pc.g1i[i] = pk(sn, cs); pc.uc[i] = FCOS(t * 0.0625f); pc.us[i] = FSIN(t * 0.0625f); }
      }
      PIN(pc.f1r); PIN(pc.f1i); PIN(pc.fr); PIN(pc.fi); PIN(pc.gr); PIN(pc.gi); PIN(pc.g1r); PIN(pc.g1i); PIN(pc.tc); PIN(pc.ts); PIN(pc.uc); PIN(pc.us);
    }
    STAMP(4);
    lsync(L, C_H, w, 4);
    if (FFT >= 2 && WGAT < 2) wimage();
    if (XDLY) __builtin_amdgcn_s_sleep(XDLY);
    pollflags<4>(O_F1H + (64 * w + lane) * F1HS, sw, tg);
    *(volatile LAS u32*)(L + C_OK + w * 4) = 1u;
    STAMP(5);
    if (FFT >= 2) {
      if (WGAT == 2) wgather();
      const int sp = 64 * w + (lane >> 1);
      const u32 xo = O_X1 + j1 * 262144 + sp * 1024 + kb * 32 + (lane & 1) * 16;
      v4u e0 = __builtin_amdgcn_raw_buffer_load_b128(rw, xo, 0, AUX_SC1), e1 = __builtin_amdgcn_raw_buffer_load_b128(rw, xo + 32768, 0, AUX_SC1);
      if (WGAT == 2) wimage();
      SPIN(s) {
        const bool bad = max(max4(e0), max4(e1)) == POISON;
        if (!__any(bad)) break;
        __builtin_amdgcn_s_sleep(4);
        FENCE();
        if (bad) { e0 = __builtin_amdgcn_raw_buffer_load_b128(rw, xo, 0, AUX_SC1); e1 = __builtin_amdgcn_raw_buffer_load_b128(rw, xo + 32768, 0, AUX_SC1); }
      }
      const u32 ep = (lane & 1) * 4096 + ((w * 16 + (sp & 15)) * 4 + ((sp >> 4) & 1)) * 4;
#pragma unroll
      for (int i = 0; i < 4; ++i) { *(LAS u32*)(L + L_E + i * 1024 + ep) = e0[i]; *(LAS u32*)(L + L_E + i * 1024 + ep + 8) = e1[i]; }
      STAMP(6);
      lsync(L, C_H, w, 5);
      v4u in = {0u, 0u, 0u, 0u};
      if (nyq) {
#pragma unroll
        for (int i = 0; i < 4; ++i) in[i] = __builtin_amdgcn_raw_buffer_load_b32(rw, O_XN + (64 * q + 16 * i + c) * 128 + j1 * 4, 0, AUX_SC1);
      }
      if (b == 0 && tid == 0) *(u64*)(p.ws + O_EPOCH) = ((u64)tg.y << 32) | tg.x;
      v4u ca = *(LAS v4u*)(L + L_E + (2 * w) * 1024 + lane * 16), cb = *(LAS v4u*)(L + L_E + (2 * w + 1) * 1024 + lane * 16);
      p2_pair(pc, ca, cb, *(LAS v4u*)(L + L_W + (2 * w) * 1024 + lane * 16), *(LAS v4u*)(L + L_W + (2 * w + 1) * 1024 + lane * 16));
      *(LAS v4u*)(L + L_O + (2 * w) * 1024 + lane * 16) = ca;
      *(LAS v4u*)(L + L_O + (2 * w + 1) * 1024 + lane * 16) = cb;
      lsync(L, C_H, w, 6);
#pragma unroll
      for (int i = 0; i < 4; ++i) { e0[i] = *(LAS u32*)(L + L_O + i * 1024 + ep); e1[i] = *(LAS u32*)(L + L_O + i * 1024 + ep + 8); }
      const u32 vo = O_V + j1 * 262144 + kb * 8192 + w * 2048;
      __builtin_amdgcn_raw_buffer_store_b128(e0, rw, lane * 16, vo, AUX_SC1);
      __builtin_amdgcn_raw_buffer_store_b128(e1, rw, lane * 16, vo + 1024, AUX_SC1);
      if (nyq) {
        SPIN(s) {
          if (!__any(max4(in) == POISON)) break;
          FENCE();
#pragma unroll
          for (int i = 0; i < 4; ++i) in[i] = __builtin_amdgcn_raw_buffer_load_b32(rw, O_XN + (64 * q + 16 * i + c) * 128 + j1 * 4, 0, AUX_SC1);
        }
#pragma unroll
        for (int i = 0; i < 4; ++i) wn[i] = pk2(up(wn[i]) * 0.5f);
        v4u dummy = in;
        p2_pair(pc, in, dummy, wn, wn);
        __builtin_amdgcn_raw_buffer_store_b128(in, rw, lane * 16, O_VN + j1 * 1024, AUX_SC1);
      }
    }
    FENCE();
    STAMP(14);
    if (FFT >= 3) {
      v4u gB[4];
      f2v pw[2];
      u32 ax;
      {
        const float ap = (float)(16 * w + c), aq = ap * (1.f / 64), k0 = (float)(4 * q) * aq;
#pragma unroll
        for (int ks = 0; ks < 4; ++ks)
#pragma unroll
          for (int i = 0; i < 4; ++i) { const float t = k0 + (float)(16 * ks + i) * aq; gB[ks][i] = pk(FCOS(t), FSIN(-t)); }
        const float t = (float)(q * (c >> 1)) * 0.125f - ((c & 1) ? 0.25f : 0.f);
        ax = pk2(f2v{FCOS(t), FSIN(-t)} * 0x1p-19f);
#pragma unroll
        for (int e = 0; e < 2; ++e) { const float u = ap * (float)(2 * q + e) * (1.f / 512); pw[e] = f2v{FCOS(u), FSIN(u)}; }
        PIN(gB[0]); PIN(gB[1]); PIN(gB[2]); PIN(gB[3]); PIN(ax); PIN(pw[0]); PIN(pw[1]);
      }
      waitv<0>();
      STAMP(7);
      lsig(L, C_H, w, 7);
      if (w == 1) { lwait(L, C_H, 7); if (lane == 0) __builtin_amdgcn_raw_buffer_store_b64(tg, rw, O_F3 + b * F3S, 0, AUX_SC1); }
      lwait<2>(L, C_GW, P3AT);
      STAMP(13);
      pollflags<VSLEEP>(O_F3 + ((2 * w + 8 * q + (c >> 3)) * 8 + (c & 7)) * F3S, sw, tg);
      const u32 vo = O_V + (2 * w + 8 * q + (c >> 3)) * 8192 + s0 * 32 + (c & 7) * 4;
      u32 vv[8], nv = 0;
#pragma unroll
      for (int j = 0; j < 8; ++j) vv[j] = __builtin_amdgcn_raw_buffer_load_b32(rw, vo, j * 262144, AUX_SC1);
      if (w == 3) nv = __builtin_amdgcn_raw_buffer_load_b32(rw, O_VN + (lane & 7) * 1024 + (((s0 >> 6) * 16 + (s0 & 15)) * 4 + ((s0 >> 4) & 3)) * 4, 0, AUX_SC1);
      STAMP(8);
      f2v a[8], y[8];
#pragma unroll
      for (int j = 0; j < 8; ++j) a[j] = up(vv[j]);
      dft8<1>(a, y);
      v4u yq[2];
#pragma unroll
      for (int i = 0; i < 8; ++i) yq[i >> 2][i & 3] = pk2(y[i]);
      const f4v z = {0.f, 0.f, 0.f, 0.f};
      const u32 uw = L_U + (2 * q) * 272 + (16 * w + c) * 4;
#pragma unroll
      for (int i = 0; i < 8; ++i) {
        v4u A = {0u, 0u, 0u, 0u};
        A[i & 3] = ax;
        const f4v d = mfma16(A, yq[i >> 2], z);
        const f2v d0 = {d[0], d[1]}, d1 = {d[2], d[3]};
        *(LAS u32*)(L + uw + i * 2176) = pk2(__builtin_elementwise_fma(f2v{d0.y, d0.x}, f2v{-pw[0].y, pw[0].y}, d0 * pw[0].x));
        *(LAS u32*)(L + uw + i * 2176 + 272) = pk2(__builtin_elementwise_fma(f2v{d1.y, d1.x}, f2v{-pw[1].y, pw[1].y}, d1 * pw[1].x));
      }
      if (w == 3) {
#pragma unroll
        for (int j = 0; j < 8; ++j) a[j] = up((u32)__builtin_amdgcn_readlane(nv, j));
        dft8<1>(a, y);
        *(LAS f4v*)(L + C_NY) = f4v{y[0].x, y[1].x, y[2].x, y[3].x} * 0x1p-19f;
        *(LAS f4v*)(L + C_NY + 16) = f4v{y[4].x, y[5].x, y[6].x, y[7].x} * 0x1p-19f;
      }
      lsync(L, C_H, w, 8);
      const u32 uo = L_U + (c >> 3) * 2176 + (c & 7) * 272 + q * 16;
      const u32 go = O_GA + (s0 + 256 * (q >> 1)) * 1024 + ((16 * w + c) * 8 + 4 * (q & 1)) * 2;
#pragma unroll
      for (int pi = 0; pi < 4; ++pi) {
        const float nyv = *(LAS float*)(L + C_NY + (2 * pi + (q >> 1)) * 4), g = *(LAS float*)(L + C_G + (2 * pi + (q >> 1)) * 4);
        f4v d = {nyv, -nyv, nyv, -nyv};
#pragma unroll
        for (int ks = 0; ks < 4; ++ks) d = mfma16(*(LAS v4u*)(L + uo + pi * 4352 + ks * 64), gB[ks], d);
        const v2u o = {pk2(up(pk(d[0], d[1])) * g), pk2(up(pk(d[2], d[3])) * g)};
        __builtin_amdgcn_raw_buffer_store_b64(o, rw, go, pi * 524288, AUX_SC1);
      }
    }
    waitv<0>();
    STAMP(9);
    lsig(L, C_H, w, 9);
    if (w == 1) { lwait(L, C_H, 9); if (lane == 0) __builtin_amdgcn_raw_buffer_store_b64(tg, rw, O_F4 + s0 * F4S, 0, AUX_SC1); }
    {
      const int s = 64 * st + (tid >> 3);
      const u32 go = O_GA + s * 1024 + dt * 128 + (tid & 7) * 16;
      lwait<2>(L, C_GW, 5);
      pollflags<4>(lane < 16 ? O_F4 + (64 * (st & 3) + 8 * w + (lane & 7) + 32 * (lane >> 3)) * F4S : WS_BYTES, sw, tg);
      fin_ga = __builtin_amdgcn_raw_buffer_load_b128(rw, go, 0, AUX_SC1);
      const v4u g2 = __builtin_amdgcn_raw_buffer_load_b128(rw, go + 32768, 0, AUX_SC1);
      fin_om = __builtin_amdgcn_raw_buffer_load_b32(rw, O_XN + (s & 255) * 128 + 32 + (s >> 8) * 4, 0, AUX_SC1);
      const u32 o2 = __builtin_amdgcn_raw_buffer_load_b32(rw, O_XN + (s & 255) * 128 + 32 * 128 + 32 + (s >> 8) * 4, 0, AUX_SC1);
      *(LAS v4u*)(L + L_GA + tid * 16) = g2;
      *(LAS u32*)(L + L_GA + 4096 + (tid >> 3) * 4) = o2;
    }
    STAMP(10);
  } else {
    INDICES();
    if (!spare) {
      const u32 lb = lbase + L_G + w * 32768, lo = L_G + w * 32768;
      const int hh = lane >> 5, blk = (lane >> 4) & 1, tq = (lane & 15) >> 2, tp = lane & 3;
      const int t1i = 28 * xl + idx, ft = MAP1 ? 7 * (xl & 3) + (idx >> 2) : t1i >> 3, d1 = MAP1 ? 4 * (xl >> 2) + (idx & 3) : t1i & 7;
      const bool half = MAP1 || ((xl & 1) ? idx < 4 : idx >= 24);
      const int rot1 = half ? 2 * (d1 & 3) : d1, xo1 = MAP1 ? xl >> 2 : 0;
      const v4i s1 = mksrd(p.w1, 2048u * 3584u), s2 = mksrd(p.w2, 1792u * 4096u);
      u32 v1off[4];
#pragma unroll
      for (int j = 0; j < 4; ++j) v1off[j] = ((lane >> 3) ^ (j >> 1)) * 3584 + ((lane & 7) ^ ((j & 1) << 2)) * 16;
      auto gblk = [&](int bl) { return (u32)(64 * w + 8 * (((bl ^ xo1) + rot1) & 7)); };
      auto w1op = [&](int bl, int slot, int j) {
        if (bl < NTB) dma16<true>(lb + slot * 8192 + j * 1024, v1off[j & 3], s1, gblk(bl) * 3584u + ft * 128 + j * (256u * 3584u));
        else dma16<false>(lb + slot * 8192 + j * 1024, v1off[j & 3], s1, gblk(bl) * 3584u + ft * 128 + j * (256u * 3584u));
      };
      const u32 t1 = lo + tq * 1024 + ((hh ^ (tq >> 1)) * 128) + ((((w >> 1) * 4 + 2 * blk + (tp >> 1)) ^ ((tq & 1) << 2)) * 16) + 8 * (tp & 1);
      const u32 t1x = t1 ^ 64u;
      v4u wr_[24];
      sfor<4, 56>([&](auto N) {
        constexpr int n = decltype(N)::v, T = THR < 25 ? THR : 25;
        if constexpr (n == 4 && OWNA < 8) lwait(L, C_H, 2);
        if constexpr (n == 8) {
          dma4(lbase + L_B + w * 256, lane * 4, mksrd(p.b1, 3584), (64 * ft + 32 * (w >> 1)) * 2);
          if (w == 0) dma4(lbase + L_B + 1024, lane * 4, mksrd(p.b2, 4096), st * 128);
          if (OWNWAIT < 8) waitv<OWNWAIT>();
        }
        if constexpr (n >= THR) waitv<THR - 1>();
        if constexpr (n >= 7 + T && (n - 7 - T) % 8 == 0 && (n - 7 - T) / 8 < 3) {
          constexpr int bl = (n - 7 - T) / 8;
          if constexpr (THR > 25) waitv<24>();
#pragma unroll
          for (int k = 0; k < 4; ++k) {
            wr_[8 * bl + 2 * k] = trd2(L, t1 + bl * 8192 + k * 256, t1 + bl * 8192 + k * 256 + 4096);
            wr_[8 * bl + 2 * k + 1] = trd2(L, t1x + bl * 8192 + k * 256, t1x + bl * 8192 + k * 256 + 4096);
          }
          WAITL();
        }
        w1op(n >> 3, (n >> 3) & 3, n & 7);
        if constexpr (n == 15) { STAMP(13); }
      });
      STAMP(2);
      lwait1(L, C_OK + w * 4, 1);
      const v2u tg = *(LAS v2u*)(L + C_TAG);
      STAMP(3);
      const u32 hv[2] = {(u32)lane * 16 + O_H + (2 * d1 + (w & 1)) * 131072, (u32)lane * 16 + O_H + (2 * d1 + ((w & 1) ^ 1)) * 131072};
      v4u act[32][2];
      auto hload = [&](int k, int e) { ld16o((k & 3) * 1024, act[k][e], hv[e], sw, gblk(k >> 2) * 512u); };
#pragma unroll
      for (int k = 0; k < issued(-1, 32); ++k) { hload(k, 0); hload(k, 1); }
      const u32 v2off = (lane >> 3) * 4096 + (((lane & 7) ^ (((lane >> 4) & 1) << 2)) * 16);
      auto w2dma = [&](int kl) {
        u32 kg = kl + (MAP2 ? 7 * (dt & 3) + 3 * (xl >> 2) : (7 * dt) >> 1); kg = kg >= 28 ? kg - 28 : kg;
        const u32 so = (u32)(448 * w) * 4096u + st * 128 + kg * 65536u;
        if (kl < NTW2) { dma16<true>(lb + kl * 2048, v2off, s2, so); dma16<true>(lb + kl * 2048 + 1024, v2off, s2, so + 8 * 4096); }
        else { dma16<false>(lb + kl * 2048, v2off, s2, so); dma16<false>(lb + kl * 2048 + 1024, v2off, s2, so + 8 * 4096); }
      };
      f16v acc[4];
      v4u wf[2][2];
      sfor<0, 32>([&](auto K) {
        constexpr int k = decltype(K)::v, kn = k + 1;
        waitv2<ops_after<1>(k)>(act[k][0], act[k][1]);
        if constexpr (k == 0) { STAMP(4); }
        const v4u a0 = k < 12 ? wr_[(2 * k) % 24] : wf[k & 1][0], a1 = k < 12 ? wr_[(2 * k + 1) % 24] : wf[k & 1][1];
        const f16v z = {};
        constexpr int i0 = issued(k - 1, 32), i1 = issued(k, 32);
        constexpr u32 o = ((kn >> 2) & 3) * 8192 + (kn & 3) * 256;
        acc[0] = mfma32(a0, act[k][0], k ? acc[0] : z);
        SCHED();
        if constexpr (kn >= 12 && kn < 32) wf[kn & 1][0] = trd2(L, t1 + o, t1 + o + 4096);
        if constexpr (i0 < i1) hload(i0, 0);
        SCHED();
        acc[1] = mfma32(a0, act[k][1], k ? acc[1] : z);
        SCHED();
        if constexpr (kn >= 12 && kn < 32) wf[kn & 1][1] = trd2(L, t1x + o, t1x + o + 4096);
        if constexpr (i0 < i1) hload(i0, 1);
        SCHED();
        acc[2] = mfma32(a1, act[k][0], k ? acc[2] : z);
        SCHED();
        if constexpr (i0 + 1 < i1) hload(i0 + 1, 0);
        SCHED();
        acc[3] = mfma32(a1, act[k][1], k ? acc[3] : z);
        SCHED();
        if constexpr (i0 + 1 < i1) hload(i0 + 1, 1);
        if constexpr (k == 15) {
          STAMP(14);
          WAITL();
#pragma unroll
          for (int j = 0; j < 8; ++j) w1op(7, 3, j);
        }
        if constexpr (k >= W2AT && k < W2AT + W2N) w2dma(k - W2AT);
        SCHED();
      });
      STAMP(5);
#pragma unroll
      for (int k = W2N; k < W2PRE; ++k) w2dma(k);
#pragma unroll
      for (int l = 1; l < 4; ++l)
#pragma unroll
        for (int g = 0; g < 4; ++g) {
          const f4v v = {acc[l][4 * g], acc[l][4 * g + 1], acc[l][4 * g + 2], acc[l][4 * g + 3]};
          *(LAS f4v*)(L + lo + 20480 + (l - 1) * 4096 + g * 1024 + lane * 16) = v;
        }
      lsig(L, C_GW, w, 2);
#pragma unroll
      for (int k = (W2PRE > W2N ? W2PRE : W2N); k < W2PRE2; ++k) w2dma(k);
      f4v sum[4];
#pragma unroll
      for (int g = 0; g < 4; ++g) {
        const v2u bb = *(LAS v2u*)(L + L_B + w * 256 + (8 * g + 4 * hh) * 2);
        sum[g] = f4v{acc[0][4 * g], acc[0][4 * g + 1], acc[0][4 * g + 2], acc[0][4 * g + 3]} + f4v{blo(bb.x), bhi(bb.x), blo(bb.y), bhi(bb.y)};
      }
      lwait(L, C_GW, 2);
#pragma unroll
      for (int l = 1; l < 4; ++l)
#pragma unroll
        for (int g = 0; g < 4; ++g) sum[g] += *(LAS f4v*)(L + L_G + (w ^ l) * 32768 + 20480 + (l - 1) * 4096 + g * 1024 + lane * 16);
      lsig(L, C_GW, w, 3);
      v4u uf[2];
#pragma unroll
      for (int g = 0; g < 4; ++g) {
        const f4v v = sum[g];
        const f4v t = v * (v * v * -0.1029432397f - 2.3022081985f);
        const f4v e = f4v{__builtin_amdgcn_exp2f(t[0]), __builtin_amdgcn_exp2f(t[1]), __builtin_amdgcn_exp2f(t[2]), __builtin_amdgcn_exp2f(t[3])} + 1.f;
        const f4v o = v * f4v{__builtin_amdgcn_rcpf(e[0]), __builtin_amdgcn_rcpf(e[1]), __builtin_amdgcn_rcpf(e[2]), __builtin_amdgcn_rcpf(e[3])};
        uf[g >> 1][2 * (g & 1)] = pk(o[0], o[1]); uf[g >> 1][2 * (g & 1) + 1] = pk(o[2], o[3]);
      }
      const u32 uo = O_U + (u32)((2 * d1 + (w & 1)) * 112 + 4 * ft + 2 * (w >> 1)) * 1024u;
      st16s(uf[0], lane * 16, sw, uo);
      st16s(uf[1], lane * 16, sw, uo + 1024);
      STAMP(6);
      lwait(L, C_GW, 3);
      constexpr int K0a = W2PRE > W2N ? W2PRE : W2N, K0 = W2PRE2 > K0a ? W2PRE2 : K0a;
#pragma unroll
      for (int k = K0; k < K0 + 4; ++k) w2dma(k);
      waitv<8>();
      lsig(L, C_GW, w, 4);
      if (w == 0) { lwait(L, C_GW, 4); st8s(tg, lane == 0 ? O_F2 + (d1 * 28 + ft) * 128 : WS_BYTES, sw); }
      STAMP(7);
#pragma unroll
      for (int k = K0 + 4; k < 16; ++k) w2dma(k);
    } else {
      if (w == 0) dma4(lbase + L_B + 1024, lane * 4, mksrd(p.b2, 4096), st * 128);
      lwait<2>(L, C_H, 4);
      const v4i s2 = mksrd(p.w2, 1792u * 4096u);
      const u32 r0 = (u32)(112 * (4 * (idx & 3) + w)) * 4096u + xl * 512;
      __builtin_amdgcn_s_sleep(SPD);

#pragma unroll
      for (int i = 0; i < SPN; ++i) {
        if (SPNT) dma4nt(lbase + L_D, (lane >> 2) * 4096 + (lane & 3) * 128, s2, r0 + i * 65536);
        else dma4(lbase + L_D, (lane >> 2) * 4096 + (lane & 3) * 128, s2, r0 + i * 65536);
        waitv<SPW>();
      }
      if (FFT >= 3) lwait<2>(L, C_H, 7);
      __builtin_amdgcn_s_sleep(SPD2);
      lsig(L, C_GW, w, 4);
      STAMP(2);
    }
    {
    INDICES();
    const u32 lb = lbase + L_G + w * 32768, w2b = (u32)(448 * w) * 4096u + st * 128, rot2 = MAP2 ? 7 * (dt & 3) + 3 * (xl >> 2) : (7 * dt) >> 1;
    const u32 lo = L_G + w * 32768;
    const v4i s2 = mksrd(p.w2, 1792u * 4096u);
    const int hh = lane >> 5, blk = (lane >> 4) & 1, tq = (lane & 15) >> 2, tp = lane & 3;
    const u32 v2off = (lane >> 3) * 4096 + (((lane & 7) ^ (((lane >> 4) & 1) << 2)) * 16);
    auto w2dma = [&](int kl) {
      u32 kg = kl + rot2; kg = kg >= 28 ? kg - 28 : kg;
      dma16<false>(lb + (kl & 15) * 2048, v2off, s2, w2b + kg * 65536u);
      dma16<false>(lb + (kl & 15) * 2048 + 1024, v2off, s2, w2b + kg * 65536u + 8 * 4096);
    };
    const u32 t2 = lo + (4 * hh + tq) * 128 + (((2 * blk + (tp >> 1)) ^ (((tq >> 1) & 1) << 2)) * 16) + 8 * (tp & 1), t2x = t2 ^ 64u;
    if (spare) {
#pragma unroll
      for (int k = 0; k < 16; ++k) w2dma(k);
    }
    v4u wr_[24];
    sfor<0, 3>([&](auto I) {
      constexpr int i = decltype(I)::v;
      waitv<24>();
#pragma unroll
      for (int k = 4 * i; k < 4 * i + 4; ++k) { wr_[2 * k] = trd2(L, t2 + k * 2048, t2 + k * 2048 + 1024); wr_[2 * k + 1] = trd2(L, t2x + k * 2048, t2x + k * 2048 + 1024); }
      WAITL();
#pragma unroll
      for (int k = 4 * i; k < 4 * i + 4; ++k) w2dma(16 + k);
    });
    STAMP(8);
    pollflags<2>(lane < 7 ? O_F2 + (dt * 28 + 7 * w + lane) * 128 : WS_BYTES, sw, *(LAS v2u*)(L + C_TAG));
    STAMP(9);
    const u32 uv[2] = {(u32)lane * 16 + O_U + (2 * dt * 112 + 28 * w) * 1024, (u32)lane * 16 + O_U + ((2 * dt + 1) * 112 + 28 * w) * 1024};
    v4u act[28][2];
    auto uload = [&](int k, int e) {
      u32 kg = k + rot2; kg = kg >= 28 ? kg - 28 : kg;
      ld16o(0, act[k][e], uv[e], sw, kg * 1024u);
    };
#pragma unroll
    for (int k = 0; k < issued(-1, 28); ++k) { uload(k, 0); uload(k, 1); }
    f16v acc[4];
    v4u wf[2][2];
    sfor<0, 28>([&](auto K) {
      constexpr int k = decltype(K)::v, kn = k + 1;
      waitv2<ops_after<2>(k)>(act[k][0], act[k][1]);
      if constexpr (k == 0) { STAMP(10); }
      const v4u b0 = k < 12 ? wr_[(2 * k) % 24] : wf[k & 1][0], b1 = k < 12 ? wr_[(2 * k + 1) % 24] : wf[k & 1][1];
      const f16v z = {};
      constexpr int i0 = issued(k - 1, 28), i1 = issued(k, 28);
      constexpr u32 o = (kn & 15) * 2048;
      acc[0] = mfma32(act[k][0], b0, k ? acc[0] : z);
      SCHED();
      if constexpr (k == GAAT) *(volatile LAS u32*)(L + C_GW + w * 4) = 5u;
      if constexpr (kn >= 12 && kn < 28) wf[kn & 1][0] = trd2(L, t2 + o, t2 + o + 1024);
      if constexpr (i0 < i1) uload(i0, 0);
      SCHED();
      acc[2] = mfma32(act[k][1], b0, k ? acc[2] : z);
      SCHED();
      if constexpr (kn >= 12 && kn < 28) wf[kn & 1][1] = trd2(L, t2x + o, t2x + o + 1024);
      if constexpr (i0 < i1) uload(i0, 1);
      SCHED();
      acc[1] = mfma32(act[k][0], b1, k ? acc[1] : z);
      SCHED();
      if constexpr (i0 + 1 < i1) uload(i0 + 1, 0);
      SCHED();
      acc[3] = mfma32(act[k][1], b1, k ? acc[3] : z);
      SCHED();
      if constexpr (i0 + 1 < i1) uload(i0 + 1, 1);
      SCHED();
    });
    STAMP(11);
#pragma unroll
    for (int md = 0; md < 2; ++md)
#pragma unroll
      for (int nt = 0; nt < 2; ++nt)
#pragma unroll
        for (int g = 0; g < 4; ++g) {
          const f16v& a = acc[2 * md + nt];
          const f4v v = {a[4 * g], a[4 * g + 1], a[4 * g + 2], a[4 * g + 3]};
          *(LAS f4v*)(L + lo + (32 * nt + (lane & 31)) * 272 + (32 * md + 8 * g + 4 * hh) * 4) = v;
        }
    STAMP(12);
    }
  }
  wgbar();
  {
    INDICES();
    const int sl = tid >> 3;
    const u32 ro = L_G + sl * 272 + (tid & 7) * 32;
    f4v s0v = *(LAS f4v*)(L + ro), s1v = *(LAS f4v*)(L + ro + 16);
#pragma unroll
    for (int l = 1; l < 4; ++l) { s0v += *(LAS f4v*)(L + ro + l * 32768); s1v += *(LAS f4v*)(L + ro + l * 32768 + 16); }
    if (wv >= 4) { fin_ga = *(LAS v4u*)(L + L_GA + (tid - 256) * 16); fin_om = *(LAS u32*)(L + L_GA + 4096 + (sl - 32) * 4); }
    const float b2 = blo(*(LAS u16*)(L + L_B + 1024 + sl * 2)), om = __uint_as_float(fin_om);
    v4u o;
#pragma unroll
    for (int i = 0; i < 4; ++i) {
      const f2v e = i < 2 ? f2v{s0v[2 * i], s0v[2 * i + 1]} : f2v{s1v[2 * i - 4], s1v[2 * i - 3]};
      o[i] = pk2(up(fin_ga[i]) + up(pk2(up(pk2(up(pk2(e)) + b2)) * om)));
    }
    *(v4u*)(p.out + (size_t)(64 * st + sl) * 512 + dt * 64 + (tid & 7) * 8) = o;
  }
}

extern "C" void kernel_launch(void* const* d_in, const int* in_sizes, int n_in, void* d_out, int out_size, void* d_ws, size_t ws_size,
                              hipStream_t stream) {
  Params p;
  p.x = (const u16*)d_in[0]; p.wr = (const u16*)d_in[1]; p.wi = (const u16*)d_in[2];
  p.w1 = (const u16*)d_in[4]; p.b1 = (const u16*)d_in[5]; p.w2 = (const u16*)d_in[6]; p.b2 = (const u16*)d_in[7]; p.gw = (const u16*)d_in[8];
  p.out = (u16*)d_out;
  p.ws = (char*)d_ws + ((ws_size - WS_BYTES) & ~(size_t)0xFFFFF);
  (void)hipFuncSetAttribute((const void*)fused_kernel, hipFuncAttributeMaxDynamicSharedMemorySize, LDS_BYTES);
  fused_kernel<<<256, 512, LDS_BYTES, stream>>>(p);
}
```
